```python
import torch

def _softmax(x, axis=-1):
    return torch.softmax(x, dim=axis)

def _gelu(x):
    return torch.nn.functional.gelu(x, approximate='tanh')

def _silu(x):
    return torch.nn.functional.silu(x)

def _sigmoid(x):
    return torch.sigmoid(x)

def _layernorm(x, axis=-1, eps=1e-05):
    m = x.mean(dim=axis, keepdim=True)
    v = x.var(dim=axis, keepdim=True, unbiased=False)
    return (x - m) / torch.sqrt(v + eps)

def _scatter_add(tgt, idx, src, axis=0):
    return tgt.clone().index_add_(axis, idx, src)

def _segment_sum(src, seg, n):
    out = torch.zeros((n,) + src.shape[1:], dtype=src.dtype)
    return out.index_add_(0, seg, src)

def _pad(x, pw):
    flat = tuple((v for pair in reversed(pw) for v in pair))
    return torch.nn.functional.pad(x, flat)
kernel_name = 'standalone__conformer_conv_module__speech'

def setup_inputs():

    _w = lambda a: (lambda t: t.to(torch.bfloat16) if t.is_floating_point() else t)(torch.as_tensor(a))
    B, T, D, K = (2, 4000, 512, 31)
    return {'x': _w((lambda *s: torch.randn(*s, dtype=torch.bfloat16) * 0.2)(B, T, D)), 'w_pw1': _w((lambda *s: torch.randn(*s, dtype=torch.bfloat16) * 0.2)(D, 2 * D)), 'dw': _w((lambda *s: torch.randn(*s, dtype=torch.bfloat16) * 0.2)(K, D)), 'w_pw2': _w((lambda *s: torch.randn(*s, dtype=torch.bfloat16) * 0.05)(D, D))}

def reference(x, w_pw1, dw, w_pw2):
    B, T, D = x.shape
    K = dw.shape[0]
    h = x @ w_pw1
    a, b = (h[..., :D].clone(), h[..., D:].clone())
    h = a * _sigmoid(b)
    pad = K // 2
    hp = _pad(h, ((0, 0), (pad, pad), (0, 0)))
    out = torch.zeros_like(h)
    for i in range(K):
        out = out + hp[:, i:i + T, :] * dw[i][None, None, :]
    return _silu(out) @ w_pw2
```

```cpp
#include <hip/hip_runtime.h>
#include <hip/hip_bf16.h>
typedef unsigned short u16;
__device__ __forceinline__ float b2f(u16 v){ return __uint_as_float(((unsigned)v)<<16); }
__device__ __forceinline__ u16 f2b(float f){ unsigned u=__float_as_uint(f); u += 0x7fffu + ((u>>16)&1u); return (u16)(u>>16); }
constexpr int B_=2,T_=4000,D_=512,K_=31;
__global__ void k1(const u16* x,const u16* w1,u16* h){
  int row=blockIdx.x; int c=threadIdx.x;
  __shared__ float xs[D_];
  xs[c]=b2f(x[row*D_+c]); __syncthreads();
  float a=0,b=0;
  for(int k=0;k<D_;k++){ a+=xs[k]*b2f(w1[k*2*D_+c]); b+=xs[k]*b2f(w1[k*2*D_+D_+c]); }
  h[row*D_+c]=f2b(a/(1.f+__expf(-b)));
}
__global__ void k2(const u16* h,const u16* dw,const u16* w2,u16* out){
  int row=blockIdx.x; int c=threadIdx.x; int b=row/T_, t=row%T_;
  __shared__ float s[D_];
  float acc=0;
  for(int i=0;i<K_;i++){ int tt=t+i-15; if(tt>=0&&tt<T_) acc+=b2f(h[(b*T_+tt)*D_+c])*b2f(dw[i*D_+c]); }
  s[c]=b2f(f2b(acc/(1.f+__expf(-acc)))); __syncthreads();
  float o=0;
  for(int k=0;k<D_;k++) o+=s[k]*b2f(w2[k*D_+c]);
  out[row*D_+c]=f2b(o);
}
extern "C" void kernel_launch(void* const* d_in, const int* in_sizes, int n_in, void* d_out, int out_size, void* d_ws, size_t ws_size, hipStream_t stream){
  k1<<<B_*T_,D_,0,stream>>>((const u16*)d_in[0],(const u16*)d_in[1],(u16*)d_ws);
  k2<<<B_*T_,D_,0,stream>>>((const u16*)d_ws,(const u16*)d_in[2],(const u16*)d_in[3],(u16*)d_out);
}
```

```cpp
#include <hip/hip_runtime.h>

typedef unsigned u32x4 __attribute__((ext_vector_type(4)));
typedef unsigned long long u64;
#define LAS __attribute__((address_space(3)))
#define GAS __attribute__((address_space(1)))
#define DI __device__ __forceinline__

constexpr int T_ = 4000, D_ = 512;
constexpr unsigned GSTR = 17u * 16384u;
constexpr unsigned S_BYTES = 32u * GSTR;
constexpr unsigned CSTR = 768;
constexpr int LDS_BYTES = 163840;
constexpr int NSLOT = 6;
constexpr unsigned XROW = 1040;
constexpr unsigned XSLOT = 18816;
constexpr unsigned HREL = 16640;
constexpr unsigned RDY_OFF = 18688;
constexpr unsigned W1_OFF = 32768;
constexpr unsigned ST_OFF = 114688;

#define WAIT_VM(n) asm volatile("s_waitcnt vmcnt(" #n ")" ::: "memory")
#define BARRIER() asm volatile("s_barrier" ::: "memory")

DI u32x4 make_srd(const void* p, unsigned nbytes) {
  u64 a = (u64)p;
  u32x4 r; r[0] = (unsigned)a; r[1] = (unsigned)(a >> 32) & 0xffffu; r[2] = nbytes; r[3] = 0x00020000u;
  return r;
}
DI void dma16(u32x4 srd, unsigned voff, unsigned soff, unsigned lds) {
  asm volatile("s_mov_b32 m0, %0\n\ts_nop 0\n\tbuffer_load_dwordx4 %1, %2, %3 offen lds"
               :: "s"(lds), "v"(voff), "s"(srd), "s"(soff) : "memory");
}

__global__ void __launch_bounds__(512, 2)
conv_module_fused(const unsigned short* __restrict__ x, const unsigned short* __restrict__ w1,
                  const unsigned short* __restrict__ dw, const unsigned short* __restrict__ w2,
                  unsigned short* __restrict__ out, unsigned char* __restrict__ ws) {
  __shared__ __attribute__((aligned(16))) unsigned char smem[LDS_BYTES];
  const unsigned lb = (unsigned)(size_t)(LAS unsigned char*)smem;
  const int lane = threadIdx.x & 63;
  const int wave = __builtin_amdgcn_readfirstlane(threadIdx.x >> 6);
  const int bid = blockIdx.x;
  const int g = (bid & 7) * 4 + (bid >> 6);
  const int j = (bid >> 3) & 7;
  const int tile = g & 15, batch = g >> 4;
  const int t0 = tile * 256;
  const int w = wave & 3;

  const int rot = 4 * (g & 3);
  const u32x4 srd_xb = make_srd(x + (size_t)batch * T_ * D_, T_ * D_ * 2);
  {
#pragma unroll
    for (int i = 0; i < 4; ++i) {
      const int rr = 4 * wave + i;
      dma16(srd_xb, (unsigned)(t0 - 16 + rr) * 1024u + ((lane ^ (rr & 15) ^ rot) * 16), 0, lb + rr * 1024);
    }
  }
#define OPAQUE_LANE(name) int name = lane; asm volatile("" : "+v"(name) :: "memory")
  const u32x4 srd_w1 = make_srd(w1, 512u * 2048u);
  unsigned w1v[2];
  {
    OPAQUE_LANE(ln);
#pragma unroll
    for (int hgh = 0; hgh < 2; ++hgh) {
      const int rr = ln >> 4, pc = ln & 15;
      const int unit = (pc >> 1) ^ (rr | (hgh << 2));
      w1v[hgh] = rr * 2048 + ((unit >> 2) * 512 + 64 * j + 16 * (unit & 3) + 8 * (pc & 1)) * 2;
    }
  }
  auto w1_piece = [&](int ks, int i) __attribute__((always_inline)) {
    dma16(srd_w1, w1v[(i >> 1) & 1], (128 * (i >> 1) + 8 * (ks ^ rot) + 4 * (i & 1)) * 2048, lb + W1_OFF + (32 * ks + 4 * i) * 256);
  };
  auto issue_w1 = [&](int ks) __attribute__((always_inline)) {
#pragma unroll
    for (int i = 0; i < 8; ++i) w1_piece(ks, i);
  };
  w1_piece(wave, 0);
  GAS u64* ctrl = (GAS u64*)(ws + S_BYTES + (g + 1) * CSTR);
  GAS u64* prog = ctrl + 32 - CSTR / 8;
  const u64 zero = 0;
  asm volatile("global_load_dwordx2 v[164:165], %0, %1 offset:-768 sc1" :: "v"(lane == 8 ? 0 : CSTR), "s"(ctrl) : "memory", "v164", "v165");
  w1_piece(wave, 1);
  unsigned sv, w2v, w2tr, hA[6], tb, fo, ovn, st, pfx, pfw, xv, dv0, dv1;
  {
    OPAQUE_LANE(ln);
    const int c = ln >> 2, jj = ln & 3;
    const unsigned hc = lb + HREL + (16 * w + c) * 32;
    hA[0] = hc + (jj >> 1) * XSLOT + (jj & 1) * 16;
    hA[1] = hc + ((jj + 1) >> 1) * XSLOT + ((jj + 1) & 1) * 16;
    asm volatile("" : "+v"(hA[0]), "+v"(hA[1]) :: "memory");
    w1_piece(wave, 2);
#pragma unroll
    for (int u = 8; u < 12; ++u) hA[u - 6] = hA[u & 1] + (u + jj >= 12 ? ((u >> 1) - NSLOT) * (int)XSLOT : (u >> 1) * (int)XSLOT);
    asm volatile("" : "+v"(hA[2]), "+v"(hA[3]), "+v"(hA[4]), "+v"(hA[5]) :: "memory");
  }
  w1_piece(wave, 3);
  {
    OPAQUE_LANE(ln);
    const int c = ln >> 2, jj = ln & 3, q = ln >> 4;
    sv = (jj >> 1) * 16384 + (4 * j + w) * 512 + c * 32 + (jj & 1) * 16;
    w2tr = lb + ST_OFF + (4 * q + (c & 3)) * 256 + (((4 * w + jj) ^ (4 * (c & 3))) * 16);
    asm volatile("" : "+v"(sv), "+v"(w2tr) :: "memory");
  }
  w1_piece(wave, 4);
  {
    OPAQUE_LANE(ln);
    const int i16 = ln & 15, q = ln >> 4;
    w2v = (8 * w + q) * 1024 + 256 * (j >> 1) + ((i16 ^ (4 * q)) * 16);
    ovn = (t0 + 16 * (j & 1) + i16) * 1024 + (128 * (j >> 1) + 32 * w + 8 * q) * 2;
    tb = lb + ST_OFF + w * 2048 + (3 - (ln & 3)) * 32 + (ln >> 2) * 2;
    asm volatile("" : "+v"(w2v), "+v"(ovn), "+v"(tb) :: "memory");
  }
  w1_piece(wave, 5);
  {
    OPAQUE_LANE(ln);
    fo = (4 * j + w) * 8 + (ln == 8 ? 256 : CSTR);
    st = lb + 8 * ln;
    pfx = ln < 8 ? (unsigned)(t0 - 16 + 16 * 9) * 1024u + ((j * 4 + w) * 8 + ln) * 128u : 0x80000000u;
    pfw = ln < 6 ? (((bid >> 3) * 4 + w) * 6 + ln) * 128u : 0x80000000u;
    xv = (t0 - 16 + 16 * 2 + 4 * w) * 1024 + ((ln ^ rot) * 16);
    asm volatile("" : "+v"(fo), "+v"(st), "+v"(pfx), "+v"(pfw), "+v"(xv) :: "memory");
  }
  w1_piece(wave, 6);
  {
    OPAQUE_LANE(ln);
    dv1 = ((ln >> 1) + 28) * 1024 + (64 * j + 16 * w) * 2 + (ln & 1) * 16;
    dv0 = dv1 - 32 * 1024;
    asm volatile("" : "+v"(dv0), "+v"(dv1) :: "memory");
  }
  w1_piece(wave, 7);
  asm volatile("s_getpc_b64 s[92:93]\n\t"
               "41:\n\t"
               "s_and_b32 s93, s93, 0xffff\n\ts_mov_b32 s94, .Lcm_end_a-41b\n\ts_max_i32 s94, s94, .Lcm_end_b-41b\n\ts_mov_b32 s95, 0x20000\n\t"
               "buffer_load_dword v166, %0, s[92:95], 0 offen"
               :: "v"(lane < 4 ? ((j * 8 + wave) * 4 + lane) * 128 : 0x80000000u) : "memory", "scc", "s92", "s93", "s94", "s95", "v166");

  if (__builtin_expect(wave < 4, 1)) {
    const int r = lane & 15, q = lane >> 4;
    const int qq = (lane >> 2) & 3, p = lane & 3;
    const unsigned f = qq | ((q & 1) << 2);
    unsigned wtr_a = lb + W1_OFF + (8 * q + qq) * 256 + ((w ^ f) * 32) + 8 * p;
    unsigned wtr_b = lb + W1_OFF + (8 * q + qq) * 256 + (((4 + w) ^ f) * 32) + 8 * p;
    const unsigned hb = lb + HREL + (16 * w + r) * 32 + 8 * q;
    const unsigned xs = lb + r * 1024 + q * 256 + r * 16;
    const unsigned xo = lb + r * XROW + q * 256;

    const u32x4 srd_s = make_srd(ws, S_BYTES);
    const unsigned rw = lb + RDY_OFF + 4 * w, l16 = lane * 16;
    const unsigned shw = lb + HREL + (19 % NSLOT) * XSLOT + w * 512;
    const u32x4 srd_h = make_srd(ws + (g + 1) * GSTR + 16 * 16384 + j * 2048 + w * 512, tile < 15 ? 512 : 0);
    const unsigned po = CSTR + ((lane == 9 && tile < 15) ? 256 + (4 * j + w) * 8 : (8 * w + (lane & 7)) * 8);
    const unsigned ssw = lb + w * 4096, sld = g * GSTR + (j & 1) * 16384 + w * 4096;
    unsigned tlo, thi;
    unsigned xa = xo + 2 * XSLOT;
    unsigned sn = 2 * XSLOT;
    unsigned cnt = 7;
    const float nl2e = -1.44269504088896f, nln2 = -0.693147180559945f;
    asm volatile(
      "s_waitcnt vmcnt(1)\n\t"
      "s_barrier\n\t"
      "v_xor_b32 v172, 0, %[xs]\n\t"
      "ds_read_b64_tr_b16 v[0:1], %[wta] offset:0\n\t"
      "ds_read_b64_tr_b16 v[2:3], %[wta] offset:1024\n\t"
      "ds_read_b64_tr_b16 v[4:5], %[wtb] offset:0\n\t"
      "ds_read_b64_tr_b16 v[6:7], %[wtb] offset:1024\n\t"
      "ds_read_b128 v[192:195], v172\n\t"
      "ds_read_b128 v[196:199], v172 offset:16384\n\t"
      "v_xor_b32 v173, 16, %[xs]\n\t"
      "ds_read_b64_tr_b16 v[8:9], %[wta] offset:8192\n\t"
      "ds_read_b64_tr_b16 v[10:11], %[wta] offset:9216\n\t"
      "ds_read_b64_tr_b16 v[12:13], %[wtb] offset:8192\n\t"
      "ds_read_b64_tr_b16 v[14:15], %[wtb] offset:9216\n\t"
      "ds_read_b128 v[200:203], v173\n\t"
      "ds_read_b128 v[204:207], v173 offset:16384\n\t"
      "v_xor_b32 v174, 32, %[xs]\n\t"
      "ds_read_b64_tr_b16 v[16:17], %[wta] offset:16384\n\t"
      "ds_read_b64_tr_b16 v[18:19], %[wta] offset:17408\n\t"
      "ds_read_b64_tr_b16 v[20:21], %[wtb] offset:16384\n\t"
      "ds_read_b64_tr_b16 v[22:23], %[wtb] offset:17408\n\t"
      "ds_read_b128 v[208:211], v174\n\t"
      "ds_read_b128 v[212:215], v174 offset:16384\n\t"
      "v_xor_b32 v175, 48, %[xs]\n\t"
      "ds_read_b64_tr_b16 v[24:25], %[wta] offset:24576\n\t"
      "ds_read_b64_tr_b16 v[26:27], %[wta] offset:25600\n\t"
      "ds_read_b64_tr_b16 v[28:29], %[wtb] offset:24576\n\t"
      "ds_read_b64_tr_b16 v[30:31], %[wtb] offset:25600\n\t"
      "ds_read_b128 v[216:219], v175\n\t"
      "ds_read_b128 v[220:223], v175 offset:16384\n\t"
      "s_waitcnt lgkmcnt(0)\n\t"
      "s_barrier\n\t"
      "v_mfma_f32_16x16x32_bf16 v[176:179], v[192:195], v[0:3], 0\n\t"
      "v_xor_b32 v172, 64, %[xs]\n\t"
      "ds_read_b64_tr_b16 v[32:33], %[wta] offset:32768\n\t"
      "v_mfma_f32_16x16x32_bf16 v[180:183], v[192:195], v[4:7], 0\n\t"
      "ds_read_b64_tr_b16 v[34:35], %[wta] offset:33792\n\t"
      "ds_read_b64_tr_b16 v[36:37], %[wtb] offset:32768\n\t"
      "v_mfma_f32_16x16x32_bf16 v[176:179], v[200:203], v[8:11], v[176:179]\n\t"
      "ds_read_b64_tr_b16 v[38:39], %[wtb] offset:33792\n\t"
      "ds_read_b128 v[224:227], v172\n\t"
      "v_mfma_f32_16x16x32_bf16 v[180:183], v[200:203], v[12:15], v[180:183]\n\t"
      "ds_read_b128 v[228:231], v172 offset:16384\n\t"
      "v_mfma_f32_16x16x32_bf16 v[176:179], v[208:211], v[16:19], v[176:179]\n\t"
      "v_xor_b32 v173, 80, %[xs]\n\t"
      "ds_read_b64_tr_b16 v[40:41], %[wta] offset:40960\n\t"
      "v_mfma_f32_16x16x32_bf16 v[180:183], v[208:211], v[20:23], v[180:183]\n\t"
      "ds_read_b64_tr_b16 v[42:43], %[wta] offset:41984\n\t"
      "ds_read_b64_tr_b16 v[44:45], %[wtb] offset:40960\n\t"
      "v_mfma_f32_16x16x32_bf16 v[176:179], v[216:219], v[24:27], v[176:179]\n\t"
      "ds_read_b64_tr_b16 v[46:47], %[wtb] offset:41984\n\t"
      "ds_read_b128 v[232:235], v173\n\t"
      "v_mfma_f32_16x16x32_bf16 v[180:183], v[216:219], v[28:31], v[180:183]\n\t"
      "ds_read_b128 v[236:239], v173 offset:16384\n\t"
      "v_mfma_f32_16x16x32_bf16 v[184:187], v[196:199], v[0:3], 0\n\t"
      "v_xor_b32 v174, 96, %[xs]\n\t"
      "ds_read_b64_tr_b16 v[48:49], %[wta] offset:49152\n\t"
      "v_mfma_f32_16x16x32_bf16 v[188:191], v[196:199], v[4:7], 0\n\t"
      "ds_read_b64_tr_b16 v[50:51], %[wta] offset:50176\n\t"
      "ds_read_b64_tr_b16 v[52:53], %[wtb] offset:49152\n\t"
      "v_mfma_f32_16x16x32_bf16 v[184:187], v[204:207], v[8:11], v[184:187]\n\t"
      "ds_read_b64_tr_b16 v[54:55], %[wtb] offset:50176\n\t"
      "ds_read_b128 v[240:243], v174\n\t"
      "v_mfma_f32_16x16x32_bf16 v[188:191], v[204:207], v[12:15], v[188:191]\n\t"
      "ds_read_b128 v[244:247], v174 offset:16384\n\t"
      "v_mfma_f32_16x16x32_bf16 v[184:187], v[212:215], v[16:19], v[184:187]\n\t"
      "v_xor_b32 v175, 112, %[xs]\n\t"
      "ds_read_b64_tr_b16 v[56:57], %[wta] offset:57344\n\t"
      "v_mfma_f32_16x16x32_bf16 v[188:191], v[212:215], v[20:23], v[188:191]\n\t"
      "ds_read_b64_tr_b16 v[58:59], %[wta] offset:58368\n\t"
      "ds_read_b64_tr_b16 v[60:61], %[wtb] offset:57344\n\t"
      "v_mfma_f32_16x16x32_bf16 v[184:187], v[220:223], v[24:27], v[184:187]\n\t"
      "ds_read_b64_tr_b16 v[62:63], %[wtb] offset:58368\n\t"
      "ds_read_b128 v[248:251], v175\n\t"
      "v_mfma_f32_16x16x32_bf16 v[188:191], v[220:223], v[28:31], v[188:191]\n\t"
      "ds_read_b128 v[252:255], v175 offset:16384\n\t"
      "s_waitcnt lgkmcnt(0)\n\t"
      "s_barrier\n\t"
      "v_add_u32 %[wta], 0x10000, %[wta]\n\t"
      "v_add_u32 %[wtb], 0x10000, %[wtb]\n\t"
      "v_mfma_f32_16x16x32_bf16 v[176:179], v[224:227], v[32:35], v[176:179]\n\t"
      "v_xor_b32 v172, 128, %[xs]\n\t"
      "ds_read_b64_tr_b16 v[64:65], %[wta] offset:0\n\t"
      "v_mfma_f32_16x16x32_bf16 v[180:183], v[224:227], v[36:39], v[180:183]\n\t"
      "ds_read_b64_tr_b16 v[66:67], %[wta] offset:1024\n\t"
      "ds_read_b64_tr_b16 v[68:69], %[wtb] offset:0\n\t"
      "v_mfma_f32_16x16x32_bf16 v[176:179], v[232:235], v[40:43], v[176:179]\n\t"
      "ds_read_b64_tr_b16 v[70:71], %[wtb] offset:1024\n\t"
      "ds_read_b128 v[192:195], v172\n\t"
      "v_mfma_f32_16x16x32_bf16 v[180:183], v[232:235], v[44:47], v[180:183]\n\t"
      "ds_read_b128 v[196:199], v172 offset:16384\n\t"
      "v_mfma_f32_16x16x32_bf16 v[176:179], v[240:243], v[48:51], v[176:179]\n\t"
      "v_xor_b32 v173, 144, %[xs]\n\t"
      "ds_read_b64_tr_b16 v[72:73], %[wta] offset:8192\n\t"
      "v_mfma_f32_16x16x32_bf16 v[180:183], v[240:243], v[52:55], v[180:183]\n\t"
      "ds_read_b64_tr_b16 v[74:75], %[wta] offset:9216\n\t"
      "ds_read_b64_tr_b16 v[76:77], %[wtb] offset:8192\n\t"
      "v_mfma_f32_16x16x32_bf16 v[176:179], v[248:251], v[56:59], v[176:179]\n\t"
      "ds_read_b64_tr_b16 v[78:79], %[wtb] offset:9216\n\t"
      "ds_read_b128 v[200:203], v173\n\t"
      "v_mfma_f32_16x16x32_bf16 v[180:183], v[248:251], v[60:63], v[180:183]\n\t"
      "ds_read_b128 v[204:207], v173 offset:16384\n\t"
      "v_mfma_f32_16x16x32_bf16 v[184:187], v[228:231], v[32:35], v[184:187]\n\t"
      "v_xor_b32 v174, 160, %[xs]\n\t"
      "ds_read_b64_tr_b16 v[80:81], %[wta] offset:16384\n\t"
      "v_mfma_f32_16x16x32_bf16 v[188:191], v[228:231], v[36:39], v[188:191]\n\t"
      "ds_read_b64_tr_b16 v[82:83], %[wta] offset:17408\n\t"
      "ds_read_b64_tr_b16 v[84:85], %[wtb] offset:16384\n\t"
      "v_mfma_f32_16x16x32_bf16 v[184:187], v[236:239], v[40:43], v[184:187]\n\t"
      "ds_read_b64_tr_b16 v[86:87], %[wtb] offset:17408\n\t"
      "ds_read_b128 v[208:211], v174\n\t"
      "v_mfma_f32_16x16x32_bf16 v[188:191], v[236:239], v[44:47], v[188:191]\n\t"
      "ds_read_b128 v[212:215], v174 offset:16384\n\t"
      "v_mfma_f32_16x16x32_bf16 v[184:187], v[244:247], v[48:51], v[184:187]\n\t"
      "v_xor_b32 v175, 176, %[xs]\n\t"
      "ds_read_b64_tr_b16 v[88:89], %[wta] offset:24576\n\t"
      "v_mfma_f32_16x16x32_bf16 v[188:191], v[244:247], v[52:55], v[188:191]\n\t"
      "ds_read_b64_tr_b16 v[90:91], %[wta] offset:25600\n\t"
      "ds_read_b64_tr_b16 v[92:93], %[wtb] offset:24576\n\t"
      "v_mfma_f32_16x16x32_bf16 v[184:187], v[252:255], v[56:59], v[184:187]\n\t"
      "ds_read_b64_tr_b16 v[94:95], %[wtb] offset:25600\n\t"
      "ds_read_b128 v[216:219], v175\n\t"
      "v_mfma_f32_16x16x32_bf16 v[188:191], v[252:255], v[60:63], v[188:191]\n\t"
      "ds_read_b128 v[220:223], v175 offset:16384\n\t"
      "s_waitcnt lgkmcnt(0)\n\t"
      "s_barrier\n\t"
      "v_mfma_f32_16x16x32_bf16 v[176:179], v[192:195], v[64:67], v[176:179]\n\t"
      "v_xor_b32 v172, 192, %[xs]\n\t"
      "ds_read_b64_tr_b16 v[96:97], %[wta] offset:32768\n\t"
      "v_mfma_f32_16x16x32_bf16 v[180:183], v[192:195], v[68:71], v[180:183]\n\t"
      "ds_read_b64_tr_b16 v[98:99], %[wta] offset:33792\n\t"
      "ds_read_b64_tr_b16 v[100:101], %[wtb] offset:32768\n\t"
      "v_mfma_f32_16x16x32_bf16 v[176:179], v[200:203], v[72:75], v[176:179]\n\t"
      "ds_read_b64_tr_b16 v[102:103], %[wtb] offset:33792\n\t"
      "ds_read_b128 v[224:227], v172\n\t"
      "v_mfma_f32_16x16x32_bf16 v[180:183], v[200:203], v[76:79], v[180:183]\n\t"
      "ds_read_b128 v[228:231], v172 offset:16384\n\t"
      "v_mfma_f32_16x16x32_bf16 v[176:179], v[208:211], v[80:83], v[176:179]\n\t"
      "v_xor_b32 v173, 208, %[xs]\n\t"
      "ds_read_b64_tr_b16 v[104:105], %[wta] offset:40960\n\t"
      "v_mfma_f32_16x16x32_bf16 v[180:183], v[208:211], v[84:87], v[180:183]\n\t"
      "ds_read_b64_tr_b16 v[106:107], %[wta] offset:41984\n\t"
      "ds_read_b64_tr_b16 v[108:109], %[wtb] offset:40960\n\t"
      "v_mfma_f32_16x16x32_bf16 v[176:179], v[216:219], v[88:91], v[176:179]\n\t"
      "ds_read_b64_tr_b16 v[110:111], %[wtb] offset:41984\n\t"
      "ds_read_b128 v[232:235], v173\n\t"
      "v_mfma_f32_16x16x32_bf16 v[180:183], v[216:219], v[92:95], v[180:183]\n\t"
      "ds_read_b128 v[236:239], v173 offset:16384\n\t"
      "v_mfma_f32_16x16x32_bf16 v[184:187], v[196:199], v[64:67], v[184:187]\n\t"
      "v_xor_b32 v174, 224, %[xs]\n\t"
      "ds_read_b64_tr_b16 v[112:113], %[wta] offset:49152\n\t"
      "v_mfma_f32_16x16x32_bf16 v[188:191], v[196:199], v[68:71], v[188:191]\n\t"
      "ds_read_b64_tr_b16 v[114:115], %[wta] offset:50176\n\t"
      "ds_read_b64_tr_b16 v[116:117], %[wtb] offset:49152\n\t"
      "v_mfma_f32_16x16x32_bf16 v[184:187], v[204:207], v[72:75], v[184:187]\n\t"
      "ds_read_b64_tr_b16 v[118:119], %[wtb] offset:50176\n\t"
      "ds_read_b128 v[240:243], v174\n\t"
      "v_mfma_f32_16x16x32_bf16 v[188:191], v[204:207], v[76:79], v[188:191]\n\t"
      "ds_read_b128 v[244:247], v174 offset:16384\n\t"
      "v_mfma_f32_16x16x32_bf16 v[184:187], v[212:215], v[80:83], v[184:187]\n\t"
      "v_xor_b32 v175, 240, %[xs]\n\t"
      "ds_read_b64_tr_b16 v[120:121], %[wta] offset:57344\n\t"
      "v_mfma_f32_16x16x32_bf16 v[188:191], v[212:215], v[84:87], v[188:191]\n\t"
      "ds_read_b64_tr_b16 v[122:123], %[wta] offset:58368\n\t"
      "ds_read_b64_tr_b16 v[124:125], %[wtb] offset:57344\n\t"
      "v_mfma_f32_16x16x32_bf16 v[184:187], v[220:223], v[88:91], v[184:187]\n\t"
      "ds_read_b64_tr_b16 v[126:127], %[wtb] offset:58368\n\t"
      "ds_read_b128 v[248:251], v175\n\t"
      "v_mfma_f32_16x16x32_bf16 v[188:191], v[220:223], v[92:95], v[188:191]\n\t"
      "ds_read_b128 v[252:255], v175 offset:16384\n\t"
      "v_mov_b32 %[tlo], v164\n\t"
      "v_mov_b32 %[thi], v165\n\t"
      "v_xor_b32 %[tlo], 0xd192ed03, %[tlo]\n\t"
      "v_xor_b32 %[thi], 0xd1b54a32, %[thi]\n\t"
      "s_mov_b32 s88, 0x7f4a7c15\n\t"
      "s_mov_b32 s89, 0x9e3779b9\n\t"
      "v_mul_lo_u32 %[thi], %[thi], s88\n\t"
      "v_mul_lo_u32 v170, %[tlo], s89\n\t"
      "v_mul_hi_u32 v171, %[tlo], s88\n\t"
      "v_mul_lo_u32 %[tlo], %[tlo], s88\n\t"
      "v_add3_u32 %[thi], %[thi], v170, v171\n\t"
      "v_and_b32 %[tlo], -16, %[tlo]\n\t"
      "s_waitcnt lgkmcnt(0)\n\t"
      "s_barrier\n\t"
      "v_mfma_f32_16x16x32_bf16 v[176:179], v[224:227], v[96:99], v[176:179]\n\t"
      "ds_read_b128 v[192:195], %[xa] offset:0\n\t"
      "v_mfma_f32_16x16x32_bf16 v[180:183], v[224:227], v[100:103], v[180:183]\n\t"
      "ds_read_b128 v[196:199], %[xa] offset:16\n\t"
      "v_mfma_f32_16x16x32_bf16 v[176:179], v[232:235], v[104:107], v[176:179]\n\t"
      "ds_read_b128 v[200:203], %[xa] offset:32\n\t"
      "v_mfma_f32_16x16x32_bf16 v[180:183], v[232:235], v[108:111], v[180:183]\n\t"
      "ds_read_b128 v[204:207], %[xa] offset:48\n\t"
      "v_mfma_f32_16x16x32_bf16 v[176:179], v[240:243], v[112:115], v[176:179]\n\t"
      "ds_read_b128 v[208:211], %[xa] offset:64\n\t"
      "v_mfma_f32_16x16x32_bf16 v[180:183], v[240:243], v[116:119], v[180:183]\n\t"
      "ds_read_b128 v[212:215], %[xa] offset:80\n\t"
      "v_mfma_f32_16x16x32_bf16 v[176:179], v[248:251], v[120:123], v[176:179]\n\t"
      "ds_read_b128 v[216:219], %[xa] offset:96\n\t"
      "v_mfma_f32_16x16x32_bf16 v[180:183], v[248:251], v[124:127], v[180:183]\n\t"
      "ds_read_b128 v[220:223], %[xa] offset:112\n\t"
      "v_mfma_f32_16x16x32_bf16 v[184:187], v[228:231], v[96:99], v[184:187]\n\t"
      "ds_read_b128 v[224:227], %[xa] offset:128\n\t"
      "s_nop 0\n\t"
      "v_mfma_f32_16x16x32_bf16 v[188:191], v[228:231], v[100:103], v[188:191]\n\t"
      "s_nop 0\n\t"
      "v_mfma_f32_16x16x32_bf16 v[184:187], v[236:239], v[104:107], v[184:187]\n\t"
      "ds_read_b128 v[228:231], %[xa] offset:144\n\t"
      "v_mfma_f32_16x16x32_bf16 v[188:191], v[236:239], v[108:111], v[188:191]\n\t"
      "v_mfma_f32_16x16x32_bf16 v[184:187], v[244:247], v[112:115], v[184:187]\n\t"
      "v_mul_f32 v172, %[k], v180\n\t"
      "v_mul_f32 v173, %[k], v181\n\t"
      "v_mfma_f32_16x16x32_bf16 v[188:191], v[244:247], v[116:119], v[188:191]\n\t"
      "v_mul_f32 v174, %[k], v182\n\t"
      "v_mul_f32 v175, %[k], v183\n\t"
      "v_mfma_f32_16x16x32_bf16 v[184:187], v[252:255], v[120:123], v[184:187]\n\t"
      "v_exp_f32 v172, v172\n\t"
      "v_mfma_f32_16x16x32_bf16 v[188:191], v[252:255], v[124:127], v[188:191]\n\t"
      "v_exp_f32 v173, v173\n\t"
      "v_exp_f32 v174, v174\n\t"
      "v_exp_f32 v175, v175\n\t"
      "v_fma_f32 v172, v172, %[k2], %[k2]\n\t"
      "v_fma_f32 v173, v173, %[k2], %[k2]\n\t"
      "v_fma_f32 v174, v174, %[k2], %[k2]\n\t"
      "v_fma_f32 v175, v175, %[k2], %[k2]\n\t"
      "v_rcp_f32 v172, v172\n\t"
      "v_rcp_f32 v173, v173\n\t"
      "v_rcp_f32 v174, v174\n\t"
      "v_rcp_f32 v175, v175\n\t"
      "v_mul_f32 v172, v176, v172\n\t"
      "v_mul_f32 v173, v177, v173\n\t"
      "v_mul_f32 v174, v178, v174\n\t"
      "v_mul_f32 v175, v179, v175\n\t"
      "v_cvt_pk_bf16_f32 v168, v172, v173\n\t"
      "v_cvt_pk_bf16_f32 v169, v174, v175\n\t"
      "s_waitcnt lgkmcnt(6)\n\t"
      ".p2align 5\n\t"
      "1:\n\t"
      "v_mfma_f32_16x16x32_bf16 v[176:179], v[192:195], v[0:3], 0\n\t"
      "ds_read_b128 v[232:235], %[xa] offset:160\n\t"
      "v_add_u32 v167, %[sn], %[hb]\n\t"
      "v_mfma_f32_16x16x32_bf16 v[180:183], v[192:195], v[4:7], 0\n\t"
      "s_add_u32 %[sn], %[sn], 37632\n\t"
      "s_cmp_eq_u32 %[sn], 112896\n\t"
      "v_mfma_f32_16x16x32_bf16 v[176:179], v[196:199], v[8:11], v[176:179]\n\t"
      "ds_read_b128 v[236:239], %[xa] offset:176\n\t"
      "s_cselect_b32 %[sn], 0, %[sn]\n\t"
      "v_mfma_f32_16x16x32_bf16 v[180:183], v[196:199], v[12:15], v[180:183]\n\t"
      "v_mul_f32 v172, %[k], v188\n\t"
      "v_mul_f32 v173, %[k], v189\n\t"
      "v_mfma_f32_16x16x32_bf16 v[176:179], v[200:203], v[16:19], v[176:179]\n\t"
      "ds_read_b128 v[240:243], %[xa] offset:192\n\t"
      "v_mul_f32 v174, %[k], v190\n\t"
      "v_mfma_f32_16x16x32_bf16 v[180:183], v[200:203], v[20:23], v[180:183]\n\t"
      "v_exp_f32 v172, v172\n\t"
      "v_mfma_f32_16x16x32_bf16 v[176:179], v[204:207], v[24:27], v[176:179]\n\t"
      "ds_read_b128 v[244:247], %[xa] offset:208\n\t"
      "s_waitcnt lgkmcnt(6)\n\t"
      "v_mfma_f32_16x16x32_bf16 v[180:183], v[204:207], v[28:31], v[180:183]\n\t"
      "v_exp_f32 v173, v173\n\t"
      "v_mfma_f32_16x16x32_bf16 v[176:179], v[208:211], v[32:35], v[176:179]\n\t"
      "ds_read_b128 v[248:251], %[xa] offset:224\n\t"
      "v_mul_f32 v175, %[k], v191\n\t"
      "v_mfma_f32_16x16x32_bf16 v[180:183], v[208:211], v[36:39], v[180:183]\n\t"
      "v_exp_f32 v174, v174\n\t"
      "v_mfma_f32_16x16x32_bf16 v[176:179], v[212:215], v[40:43], v[176:179]\n\t"
      "ds_read_b128 v[252:255], %[xa] offset:240\n\t"
      "v_fma_f32 v172, v172, %[k2], %[k2]\n\t"
      "v_mfma_f32_16x16x32_bf16 v[180:183], v[212:215], v[44:47], v[180:183]\n\t"
      "s_cmp_eq_u32 %[cnt], 0\n\t"
      "s_cbranch_scc1 27f\n\t"
      "28:\n\t"
      "v_mfma_f32_16x16x32_bf16 v[176:179], v[216:219], v[48:51], v[176:179]\n\t"
      "ds_read_b128 v[192:195], %[xa] offset:18816\n\t"
      "v_fma_f32 v173, v173, %[k2], %[k2]\n\t"
      "v_mfma_f32_16x16x32_bf16 v[180:183], v[216:219], v[52:55], v[180:183]\n\t"
      "v_exp_f32 v175, v175\n\t"
      "v_mfma_f32_16x16x32_bf16 v[176:179], v[220:223], v[56:59], v[176:179]\n\t"
      "ds_read_b128 v[196:199], %[xa] offset:18832\n\t"
      "s_waitcnt lgkmcnt(6)\n\t"
      "v_mfma_f32_16x16x32_bf16 v[180:183], v[220:223], v[60:63], v[180:183]\n\t"
      "v_rcp_f32 v172, v172\n\t"
      "v_mfma_f32_16x16x32_bf16 v[176:179], v[224:227], v[64:67], v[176:179]\n\t"
      "ds_read_b128 v[200:203], %[xa] offset:18848\n\t"
      "v_fma_f32 v174, v174, %[k2], %[k2]\n\t"
      "v_mfma_f32_16x16x32_bf16 v[180:183], v[224:227], v[68:71], v[180:183]\n\t"
      "v_rcp_f32 v173, v173\n\t"
      "v_mfma_f32_16x16x32_bf16 v[176:179], v[228:231], v[72:75], v[176:179]\n\t"
      "ds_read_b128 v[204:207], %[xa] offset:18864\n\t"
      "v_fma_f32 v175, v175, %[k2], %[k2]\n\t"
      "v_mfma_f32_16x16x32_bf16 v[180:183], v[228:231], v[76:79], v[180:183]\n\t"
      "v_rcp_f32 v174, v174\n\t"
      "v_mfma_f32_16x16x32_bf16 v[176:179], v[232:235], v[80:83], v[176:179]\n\t"
      "ds_read_b128 v[208:211], %[xa] offset:18880\n\t"
      "v_mul_f32 v172, v184, v172\n\t"
      "v_mfma_f32_16x16x32_bf16 v[180:183], v[232:235], v[84:87], v[180:183]\n\t"
      "v_rcp_f32 v175, v175\n\t"
      "v_mfma_f32_16x16x32_bf16 v[176:179], v[236:239], v[88:91], v[176:179]\n\t"
      "ds_read_b128 v[212:215], %[xa] offset:18896\n\t"
      "s_waitcnt lgkmcnt(6)\n\t"
      "v_mfma_f32_16x16x32_bf16 v[180:183], v[236:239], v[92:95], v[180:183]\n\t"
      "v_mul_f32 v173, v185, v173\n\t"
      "v_mul_f32 v174, v186, v174\n\t"
      "v_mfma_f32_16x16x32_bf16 v[176:179], v[240:243], v[96:99], v[176:179]\n\t"
      "ds_read_b128 v[216:219], %[xa] offset:18912\n\t"
      "v_mul_f32 v175, v187, v175\n\t"
      "v_mfma_f32_16x16x32_bf16 v[180:183], v[240:243], v[100:103], v[180:183]\n\t"
      "v_cvt_pk_bf16_f32 v170, v172, v173\n\t"
      "v_mfma_f32_16x16x32_bf16 v[176:179], v[244:247], v[104:107], v[176:179]\n\t"
      "ds_read_b128 v[220:223], %[xa] offset:18928\n\t"
      "v_cvt_pk_bf16_f32 v171, v174, v175\n\t"
      "v_mfma_f32_16x16x32_bf16 v[180:183], v[244:247], v[108:111], v[180:183]\n\t"
      "s_sub_u32 %[cnt], %[cnt], 1\n\t"
      "v_mfma_f32_16x16x32_bf16 v[176:179], v[248:251], v[112:115], v[176:179]\n\t"
      "ds_read_b128 v[224:227], %[xa] offset:18944\n\t"
      "ds_write_b64 v167, v[168:169]\n\t"
      "v_mfma_f32_16x16x32_bf16 v[180:183], v[248:251], v[116:119], v[180:183]\n\t"
      "ds_write_b64 v167, v[170:171] offset:18816\n\t"
      "v_mfma_f32_16x16x32_bf16 v[184:187], v[252:255], v[120:123], v[176:179]\n\t"
      "ds_read_b128 v[228:231], %[xa] offset:18960\n\t"
      "s_waitcnt lgkmcnt(6)\n\t"
      "v_mfma_f32_16x16x32_bf16 v[188:191], v[252:255], v[124:127], v[180:183]\n\t"
      "s_cbranch_scc1 7f\n\t"
      "v_mfma_f32_16x16x32_bf16 v[176:179], v[192:195], v[0:3], 0\n\t"
      "ds_read_b128 v[232:235], %[xa] offset:18976\n\t"
      "v_mfma_f32_16x16x32_bf16 v[180:183], v[192:195], v[4:7], 0\n\t"
      "s_cmp_eq_u32 %[cnt], 0\n\t"
      "s_cbranch_scc1 17f\n\t"
      "8:\n\t"
      "v_mfma_f32_16x16x32_bf16 v[176:179], v[196:199], v[8:11], v[176:179]\n\t"
      "ds_read_b128 v[236:239], %[xa] offset:18992\n\t"
      "v_mfma_f32_16x16x32_bf16 v[180:183], v[196:199], v[12:15], v[180:183]\n\t"
      "v_mul_f32 v172, %[k], v188\n\t"
      "v_mul_f32 v173, %[k], v189\n\t"
      "v_mfma_f32_16x16x32_bf16 v[176:179], v[200:203], v[16:19], v[176:179]\n\t"
      "ds_read_b128 v[240:243], %[xa] offset:19008\n\t"
      "v_mul_f32 v174, %[k], v190\n\t"
      "v_mfma_f32_16x16x32_bf16 v[180:183], v[200:203], v[20:23], v[180:183]\n\t"
      "v_exp_f32 v172, v172\n\t"
      "v_mfma_f32_16x16x32_bf16 v[176:179], v[204:207], v[24:27], v[176:179]\n\t"
      "ds_read_b128 v[244:247], %[xa] offset:19024\n\t"
      "s_waitcnt lgkmcnt(5)\n\t"
      "s_barrier\n\t"
      "v_mfma_f32_16x16x32_bf16 v[180:183], v[204:207], v[28:31], v[180:183]\n\t"
      "v_exp_f32 v173, v173\n\t"
      "v_mfma_f32_16x16x32_bf16 v[176:179], v[208:211], v[32:35], v[176:179]\n\t"
      "ds_read_b128 v[248:251], %[xa] offset:19040\n\t"
      "v_mul_f32 v175, %[k], v191\n\t"
      "v_mfma_f32_16x16x32_bf16 v[180:183], v[208:211], v[36:39], v[180:183]\n\t"
      "v_exp_f32 v174, v174\n\t"
      "v_mfma_f32_16x16x32_bf16 v[176:179], v[212:215], v[40:43], v[176:179]\n\t"
      "ds_read_b128 v[252:255], %[xa] offset:19056\n\t"
      "v_fma_f32 v172, v172, %[k2], %[k2]\n\t"
      "v_mfma_f32_16x16x32_bf16 v[180:183], v[212:215], v[44:47], v[180:183]\n\t"
      "v_add_u32 %[xa], %[sn], %[xo]\n\t"
      "v_mfma_f32_16x16x32_bf16 v[176:179], v[216:219], v[48:51], v[176:179]\n\t"
      "ds_read_b128 v[192:195], %[xa] offset:0\n\t"
      "v_fma_f32 v173, v173, %[k2], %[k2]\n\t"
      "v_mfma_f32_16x16x32_bf16 v[180:183], v[216:219], v[52:55], v[180:183]\n\t"
      "v_exp_f32 v175, v175\n\t"
      "v_mfma_f32_16x16x32_bf16 v[176:179], v[220:223], v[56:59], v[176:179]\n\t"
      "ds_read_b128 v[196:199], %[xa] offset:16\n\t"
      "s_waitcnt lgkmcnt(6)\n\t"
      "v_mfma_f32_16x16x32_bf16 v[180:183], v[220:223], v[60:63], v[180:183]\n\t"
      "v_rcp_f32 v172, v172\n\t"
      "v_mfma_f32_16x16x32_bf16 v[176:179], v[224:227], v[64:67], v[176:179]\n\t"
      "ds_read_b128 v[200:203], %[xa] offset:32\n\t"
      "v_fma_f32 v174, v174, %[k2], %[k2]\n\t"
      "v_mfma_f32_16x16x32_bf16 v[180:183], v[224:227], v[68:71], v[180:183]\n\t"
      "v_rcp_f32 v173, v173\n\t"
      "v_mfma_f32_16x16x32_bf16 v[176:179], v[228:231], v[72:75], v[176:179]\n\t"
      "ds_read_b128 v[204:207], %[xa] offset:48\n\t"
      "v_fma_f32 v175, v175, %[k2], %[k2]\n\t"
      "v_mfma_f32_16x16x32_bf16 v[180:183], v[228:231], v[76:79], v[180:183]\n\t"
      "v_rcp_f32 v174, v174\n\t"
      "v_mfma_f32_16x16x32_bf16 v[176:179], v[232:235], v[80:83], v[176:179]\n\t"
      "ds_read_b128 v[208:211], %[xa] offset:64\n\t"
      "v_mul_f32 v172, v184, v172\n\t"
      "v_mfma_f32_16x16x32_bf16 v[180:183], v[232:235], v[84:87], v[180:183]\n\t"
      "v_rcp_f32 v175, v175\n\t"
      "v_mfma_f32_16x16x32_bf16 v[176:179], v[236:239], v[88:91], v[176:179]\n\t"
      "ds_read_b128 v[212:215], %[xa] offset:80\n\t"
      "s_waitcnt lgkmcnt(6)\n\t"
      "v_mfma_f32_16x16x32_bf16 v[180:183], v[236:239], v[92:95], v[180:183]\n\t"
      "v_mul_f32 v173, v185, v173\n\t"
      "v_mul_f32 v174, v186, v174\n\t"
      "v_mfma_f32_16x16x32_bf16 v[176:179], v[240:243], v[96:99], v[176:179]\n\t"
      "ds_read_b128 v[216:219], %[xa] offset:96\n\t"
      "v_mul_f32 v175, v187, v175\n\t"
      "v_mfma_f32_16x16x32_bf16 v[180:183], v[240:243], v[100:103], v[180:183]\n\t"
      "v_cvt_pk_bf16_f32 v168, v172, v173\n\t"
      "v_mfma_f32_16x16x32_bf16 v[176:179], v[244:247], v[104:107], v[176:179]\n\t"
      "ds_read_b128 v[220:223], %[xa] offset:112\n\t"
      "v_cvt_pk_bf16_f32 v169, v174, v175\n\t"
      "v_mfma_f32_16x16x32_bf16 v[180:183], v[244:247], v[108:111], v[180:183]\n\t"
      "v_mfma_f32_16x16x32_bf16 v[176:179], v[248:251], v[112:115], v[176:179]\n\t"
      "ds_read_b128 v[224:227], %[xa] offset:128\n\t"
      "v_mfma_f32_16x16x32_bf16 v[180:183], v[248:251], v[116:119], v[180:183]\n\t"
      "v_mfma_f32_16x16x32_bf16 v[184:187], v[252:255], v[120:123], v[176:179]\n\t"
      "ds_read_b128 v[228:231], %[xa] offset:144\n\t"
      "s_waitcnt lgkmcnt(6)\n\t"
      "v_mfma_f32_16x16x32_bf16 v[188:191], v[252:255], v[124:127], v[180:183]\n\t"
      "s_branch 1b\n\t"
      "17:\n\t"
      "s_mov_b64 exec, 0x2ff\n\t"
      "global_atomic_add_x2 v[164:165], %[po], %[z], %[prog] sc0\n\t"
      "s_mov_b64 exec, -1\n\t"
      "s_branch 8b\n\t"
      "27:\n\t"
      "s_mov_b64 exec, 0x2ff\n\t"
      "global_atomic_add_x2 v[162:163], %[po], %[z], %[prog] sc0\n\t"
      "s_mov_b64 exec, -1\n\t"
      "s_branch 28b\n\t"
      "7:\n\t"
      "s_mov_b32 s86, 0\n\t"
      "s_mov_b32 s87, 0x80000\n\t"
      "s_waitcnt lgkmcnt(1)\n\t"
      "s_barrier\n\t"
      "s_waitcnt vmcnt(1)\n\t"
      "s_branch 15f\n\t"
      "13:\n\t"
      "s_waitcnt vmcnt(0)\n\t"
      "15:\n\t"
      "v_sub_u32 v164, v164, %[tlo]\n\t"
      "v_cmp_eq_u32 vcc, v165, %[thi]\n\t"
      "v_cmp_ge_u32 s[82:83], 8, v164\n\t"
      "s_and_b64 vcc, vcc, s[82:83]\n\t"
      "v_cndmask_b32 v250, 0, v164, vcc\n\t"
      "v_cmp_lt_u32 vcc, 1, v250\n\t"
      "s_and_b32 s82, vcc_lo, 0x2ff\n\t"
      "s_cmp_eq_u32 s82, 0x2ff\n\t"
      "s_cbranch_scc1 14f\n\t"
      "s_mov_b64 exec, 0x2ff\n\t"
      "global_atomic_add_x2 v[164:165], %[po], %[z], %[prog] sc0\n\t"
      "s_mov_b64 exec, -1\n\t"
      "s_sub_u32 s87, s87, 1\n\t"
      "s_cbranch_scc0 13b\n\t"
      "14:\n\t"
      "s_mov_b32 m0, %[shw]\n\t"
      "s_mov_b32 exec_hi, 0\n\t"
      "buffer_load_dwordx4 %[l16], %[srdh], 0 offen sc1 lds\n\t"
      "s_mov_b64 exec, -1\n\t"
      "v_mul_f32 v172, %[k], v188\n\t"
      "v_mul_f32 v173, %[k], v189\n\t"
      "v_mul_f32 v174, %[k], v190\n\t"
      "v_mul_f32 v175, %[k], v191\n\t"
      "v_exp_f32 v172, v172\n\t"
      "v_exp_f32 v173, v173\n\t"
      "v_exp_f32 v174, v174\n\t"
      "v_exp_f32 v175, v175\n\t"
      "v_fma_f32 v172, v172, %[k2], %[k2]\n\t"
      "v_fma_f32 v173, v173, %[k2], %[k2]\n\t"
      "v_fma_f32 v174, v174, %[k2], %[k2]\n\t"
      "v_fma_f32 v175, v175, %[k2], %[k2]\n\t"
      "v_rcp_f32 v172, v172\n\t"
      "v_rcp_f32 v173, v173\n\t"
      "v_rcp_f32 v174, v174\n\t"
      "v_rcp_f32 v175, v175\n\t"
      "v_mul_f32 v172, v184, v172\n\t"
      "v_mul_f32 v173, v185, v173\n\t"
      "v_mul_f32 v174, v186, v174\n\t"
      "v_mul_f32 v175, v187, v175\n\t"
      "v_cvt_pk_bf16_f32 v170, v172, v173\n\t"
      "v_cvt_pk_bf16_f32 v171, v174, v175\n\t"
      "ds_write_b64 %[hb], v[170:171] offset:0\n\t"
      "v_mov_b32 v251, 2\n\t"
      "20:\n\t"
      "v_cmp_lt_u32 vcc, s86, v251\n\t"
      "s_and_b32 s82, vcc_lo, 0xff\n\t"
      "s_cmp_eq_u32 s82, 0xff\n\t"
      "s_cbranch_scc0 21f\n\t"
      "s_mul_i32 s88, s86, 18816\n\t"
      "s_add_u32 m0, s88, %[ssw]\n\t"
      "s_lshl_b32 s88, s86, 15\n\t"
      "s_add_u32 s88, s88, %[sld]\n\t"
      "buffer_load_dwordx4 %[l16], %[srds], s88 offen offset:0 sc1 lds\n\t"
      "buffer_load_dwordx4 %[l16], %[srds], s88 offen offset:1024 sc1 lds\n\t"
      "buffer_load_dwordx4 %[l16], %[srds], s88 offen offset:2048 sc1 lds\n\t"
      "buffer_load_dwordx4 %[l16], %[srds], s88 offen offset:3072 sc1 lds\n\t"
      "s_add_u32 s86, s86, 1\n\t"
      "s_branch 20b\n\t"
      "21:\n\t"
      "s_waitcnt vmcnt(8) lgkmcnt(0)\n\t"
      "s_barrier\n\t"
      "v_mov_b32 v253, v250\n\t"
      "v_mov_b32 v164, v162\n\t"
      "v_mov_b32 v165, v163\n\t"
      "v_sub_u32 v164, v164, %[tlo]\n\t"
      "v_cmp_eq_u32 vcc, v165, %[thi]\n\t"
      "v_cmp_ge_u32 s[82:83], 8, v164\n\t"
      "s_and_b64 vcc, vcc, s[82:83]\n\t"
      "v_cndmask_b32 v250, 0, v164, vcc\n\t"
      "v_max_u32 v250, v250, v253\n\t"
      "v_min_u32 v251, 3, v250\n\t"
      "22:\n\t"
      "v_cmp_lt_u32 vcc, s86, v251\n\t"
      "s_and_b32 s82, vcc_lo, 0xff\n\t"
      "s_cmp_eq_u32 s82, 0xff\n\t"
      "s_cbranch_scc0 23f\n\t"
      "s_mul_i32 s88, s86, 18816\n\t"
      "s_add_u32 m0, s88, %[ssw]\n\t"
      "s_lshl_b32 s88, s86, 15\n\t"
      "s_add_u32 s88, s88, %[sld]\n\t"
      "buffer_load_dwordx4 %[l16], %[srds], s88 offen offset:0 sc1 lds\n\t"
      "buffer_load_dwordx4 %[l16], %[srds], s88 offen offset:1024 sc1 lds\n\t"
      "buffer_load_dwordx4 %[l16], %[srds], s88 offen offset:2048 sc1 lds\n\t"
      "buffer_load_dwordx4 %[l16], %[srds], s88 offen offset:3072 sc1 lds\n\t"
      "s_add_u32 s86, s86, 1\n\t"
      "s_branch 22b\n\t"
      "23:\n\t"
      "s_cmp_eq_u32 s86, 3\n\t"
      "s_cbranch_scc1 24f\n\t"
      "s_waitcnt vmcnt(0)\n\t"
      "24:\n\t"
      "s_waitcnt vmcnt(4)\n\t"
      "s_barrier\n\t"
      "s_branch 11f\n\t"
      "10:\n\t"
      "s_waitcnt vmcnt(1)\n\t"
      "v_mov_b32 v252, s86\n\t"
      "ds_write_b32 %[rw], v252\n\t"
      "s_waitcnt vmcnt(0)\n\t"
      "v_sub_u32 v164, v164, %[tlo]\n\t"
      "v_cmp_eq_u32 vcc, v165, %[thi]\n\t"
      "v_cmp_ge_u32 s[82:83], 8, v164\n\t"
      "s_and_b64 vcc, vcc, s[82:83]\n\t"
      "v_cndmask_b32 v250, 0, v164, vcc\n\t"
      "11:\n\t"
      "v_cmp_lt_u32 vcc, s86, v250\n\t"
      "s_and_b32 s82, vcc_lo, 0xff\n\t"
      "s_cmp_eq_u32 s82, 0xff\n\t"
      "s_cbranch_scc0 12f\n\t"
      "s_mul_i32 s88, s86, 18816\n\t"
      "s_add_u32 m0, s88, %[ssw]\n\t"
      "s_lshl_b32 s88, s86, 15\n\t"
      "s_add_u32 s88, s88, %[sld]\n\t"
      "buffer_load_dwordx4 %[l16], %[srds], s88 offen offset:0 sc1 lds\n\t"
      "buffer_load_dwordx4 %[l16], %[srds], s88 offen offset:1024 sc1 lds\n\t"
      "buffer_load_dwordx4 %[l16], %[srds], s88 offen offset:2048 sc1 lds\n\t"
      "buffer_load_dwordx4 %[l16], %[srds], s88 offen offset:3072 sc1 lds\n\t"
      "s_add_u32 s86, s86, 1\n\t"
      "s_branch 11b\n\t"
      "12:\n\t"
      "s_cmp_eq_u32 s86, 8\n\t"
      "s_cbranch_scc1 18f\n\t"
      "s_mov_b64 exec, 0x2ff\n\t"
      "global_atomic_add_x2 v[164:165], %[po], %[z], %[prog] sc0\n\t"
      "s_mov_b64 exec, -1\n\t"
      "s_sub_u32 s87, s87, 1\n\t"
      "s_cbranch_scc0 10b\n\t"
      "18:\n\t"
      "s_waitcnt vmcnt(4)\n\t"
      "v_mov_b32 v252, 7\n\t"
      "ds_write_b32 %[rw], v252\n\t"
      "s_waitcnt vmcnt(0)\n\t"
      "v_mov_b32 v252, 8\n\t"
      "ds_write_b32 %[rw], v252\n\t"
      ".Lcm_end_a:\n\t"
      : [xa] "+v"(xa), [sn] "+s"(sn), [cnt] "+s"(cnt), [wta] "+v"(wtr_a), [wtb] "+v"(wtr_b), [tlo] "=&v"(tlo), [thi] "=&v"(thi)
      : [xo] "v"(xo), [xs] "v"(xs), [hb] "v"(hb), [k] "s"(nl2e), [k2] "s"(nln2), [rw] "v"(rw), [l16] "v"(l16), [po] "v"(po), [z] "v"(zero), [srds] "s"(srd_s), [prog] "s"(prog), [ssw] "s"(ssw), [sld] "s"(sld), [shw] "s"(shw), [srdh] "s"(srd_h)
      : "memory", "scc", "vcc", "s82", "s83", "s84", "s85", "s86", "s87", "s88", "s89", "s90", "s91", "v0", "v1", "v2", "v3", "v4", "v5", "v6", "v7", "v8", "v9", "v10", "v11", "v12", "v13", "v14", "v15", "v16", "v17", "v18", "v19", "v20", "v21", "v22", "v23", "v24", "v25", "v26", "v27", "v28", "v29", "v30", "v31", "v32", "v33", "v34", "v35", "v36", "v37", "v38", "v39", "v40", "v41", "v42", "v43", "v44", "v45", "v46", "v47", "v48", "v49", "v50", "v51", "v52", "v53", "v54", "v55", "v56", "v57", "v58", "v59", "v60", "v61", "v62", "v63", "v64", "v65", "v66", "v67", "v68", "v69", "v70", "v71", "v72", "v73", "v74", "v75", "v76", "v77", "v78", "v79", "v80", "v81", "v82", "v83", "v84", "v85", "v86", "v87", "v88", "v89", "v90", "v91", "v92", "v93", "v94", "v95", "v96", "v97", "v98", "v99", "v100", "v101", "v102", "v103", "v104", "v105", "v106", "v107", "v108", "v109", "v110", "v111", "v112", "v113", "v114", "v115", "v116", "v117", "v118", "v119", "v120", "v121", "v122", "v123", "v124", "v125", "v126", "v127", "v162", "v163", "v164", "v165", "v166", "v167", "v168", "v169", "v170", "v171", "v172", "v173", "v174", "v175", "v176", "v177", "v178", "v179", "v180", "v181", "v182", "v183", "v184", "v185", "v186", "v187", "v188", "v189", "v190", "v191", "v192", "v193", "v194", "v195", "v196", "v197", "v198", "v199", "v200", "v201", "v202", "v203", "v204", "v205", "v206", "v207", "v208", "v209", "v210", "v211", "v212", "v213", "v214", "v215", "v216", "v217", "v218", "v219", "v220", "v221", "v222", "v223", "v224", "v225", "v226", "v227", "v228", "v229", "v230", "v231", "v232", "v233", "v234", "v235", "v236", "v237", "v238", "v239", "v240", "v241", "v242", "v243", "v244", "v245", "v246", "v247", "v248", "v249", "v250", "v251", "v252", "v253", "v254", "v255");
    __builtin_amdgcn_endpgm();
  } else {
    const unsigned sxw = lb + 4 * w * XROW, sww = lb + ST_OFF + w * 2048;
    auto issue_x = [&](int m) __attribute__((always_inline)) {
#pragma unroll
      for (int i = 0; i < 4; ++i) dma16(srd_xb, xv, i * 1024, sxw + (m % NSLOT) * XSLOT + i * XROW);
      xv += 16384;
    };
    WAIT_VM(10); BARRIER();
    issue_w1(8 + w);
    WAIT_VM(8); BARRIER();
    issue_w1(12 + w); issue_x(2);
    WAIT_VM(12); BARRIER();
    issue_x(3);
    WAIT_VM(8); BARRIER();
    const u32x4 srd_dw = make_srd(dw, 31u * 1024u);
    dma16(srd_dw, dv0, 0, sww); dma16(srd_dw, dv1, 0, sww + 1024);
    {
      unsigned o0 = (batch * T_ + t0 + 80) * 1024 + (j * 2 + w) * 3072, o1 = 65536 + ((bid >> 3) * 2 + w - 2) * 1024;
      asm("" : "+s"(o0), "+s"(o1));
      const char* p = (const char*)((w < 2 ? (u64)x : (u64)w2) + (w < 2 ? o0 : o1));
      asm volatile("buffer_load_dword v190, %0, %1, 0 offen" :: "v"((st - lb) * 16), "s"(make_srd(p, w < 2 ? 3072 : 1024)) : "memory", "v190");
    }
    const u32x4 srd_s = make_srd(ws, S_BYTES);
    const u32x4 srd_w2 = make_srd(w2, 512u * 1024u);
    const u32x4 srd_o = make_srd(out + (size_t)batch * T_ * D_, T_ * D_ * 2);
    const unsigned sst = g * GSTR, rp = lb + RDY_OFF, rw = rp + 4 * w;
    unsigned ov = 0x80000000u;
    const unsigned l16 = (st - lb) * 2;
    const u64 fm = tile > 0 ? 0x101 : 1;
    const unsigned shw = lb + HREL + w * 512, sho = g * GSTR + 16 * 16384 + j * 2048 + w * 512;
    const float nl2e = -1.44269504088896f;
    WAIT_VM(3);
    unsigned tlo, thi;
    asm volatile(
      "s_barrier\n\t"
      "s_add_u32 m0, %[sxw], 75264\n\t"
      "s_nop 0\n\t"
      "buffer_load_dwordx4 %[xv], %[srdx], 0 offen offset:0 lds\n\t"
      "s_nop 0\n\t"
      "v_xor_b32 %[tlo], 0xd192ed03, v164\n\t"
      "v_xor_b32 %[thi], 0xd1b54a32, v165\n\t"
      "s_mov_b32 s86, 0x7f4a7c15\n\t"
      "s_mov_b32 s87, 0x9e3779b9\n\t"
      "s_add_u32 m0, %[sxw], 75280\n\t"
      "s_nop 0\n\t"
      "buffer_load_dwordx4 %[xv], %[srdx], 0 offen offset:1024 lds\n\t"
      "v_mul_lo_u32 %[thi], %[thi], s86\n\t"
      "v_mul_lo_u32 v174, %[tlo], s87\n\t"
      "v_mul_hi_u32 v175, %[tlo], s86\n\t"
      "s_add_u32 m0, %[sxw], 75296\n\t"
      "s_nop 0\n\t"
      "buffer_load_dwordx4 %[xv], %[srdx], 0 offen offset:2048 lds\n\t"
      "v_mul_lo_u32 %[tlo], %[tlo], s86\n\t"
      "v_add3_u32 %[thi], %[thi], v174, v175\n\t"
      "v_and_b32 %[tlo], -16, %[tlo]\n\t"
      "s_add_u32 m0, %[sxw], 75312\n\t"
      "s_nop 0\n\t"
      "buffer_load_dwordx4 %[xv], %[srdx], 0 offen offset:3072 lds\n\t"
      "v_add_u32 %[xv], 0x4000, %[xv]\n\t"
      "s_waitcnt vmcnt(4)\n\t"
      "ds_read_u16 v146, %[tb] offset:0\n\t"
      "ds_read_u16 v147, %[tb] offset:32\n\t"
      "ds_read_u16 v148, %[tb] offset:64\n\t"
      "s_add_u32 m0, %[sxw], 94080\n\t"
      "s_nop 0\n\t"
      "buffer_load_dwordx4 %[xv], %[srdx], 0 offen offset:0 lds\n\t"
      "ds_read_u16 v149, %[tb] offset:96\n\t"
      "ds_read_u16 v150, %[tb] offset:128\n\t"
      "ds_read_u16 v151, %[tb] offset:160\n\t"
      "s_add_u32 m0, %[sxw], 94096\n\t"
      "s_nop 0\n\t"
      "buffer_load_dwordx4 %[xv], %[srdx], 0 offen offset:1024 lds\n\t"
      "ds_read_u16 v152, %[tb] offset:192\n\t"
      "ds_read_u16 v153, %[tb] offset:224\n\t"
      "ds_read_u16 v154, %[tb] offset:256\n\t"
      "s_add_u32 m0, %[sxw], 94112\n\t"
      "s_nop 0\n\t"
      "buffer_load_dwordx4 %[xv], %[srdx], 0 offen offset:2048 lds\n\t"
      "ds_read_u16 v155, %[tb] offset:288\n\t"
      "ds_read_u16 v156, %[tb] offset:320\n\t"
      "ds_read_u16 v157, %[tb] offset:352\n\t"
      "s_add_u32 m0, %[sxw], 94128\n\t"
      "s_nop 0\n\t"
      "buffer_load_dwordx4 %[xv], %[srdx], 0 offen offset:3072 lds\n\t"
      "v_add_u32 %[xv], 0x4000, %[xv]\n\t"
      "ds_read_u16 v158, %[tb] offset:384\n\t"
      "ds_read_u16 v159, %[tb] offset:416\n\t"
      "ds_read_u16 v160, %[tb] offset:448\n\t"
      "s_add_u32 m0, %[sxw], 0\n\t"
      "s_nop 0\n\t"
      "buffer_load_dwordx4 %[xv], %[srdx], 0 offen offset:0 lds\n\t"
      "ds_read_u16 v161, %[tb] offset:480\n\t"
      "ds_read_u16 v162, %[tb] offset:512\n\t"
      "ds_read_u16 v163, %[tb] offset:544\n\t"
      "s_add_u32 m0, %[sxw], 16\n\t"
      "s_nop 0\n\t"
      "buffer_load_dwordx4 %[xv], %[srdx], 0 offen offset:1024 lds\n\t"
      "s_waitcnt lgkmcnt(0)\n\t"
      "v_lshl_or_b32 v128, v147, 16, v146\n\t"
      "v_lshl_or_b32 v129, v149, 16, v148\n\t"
      "s_add_u32 m0, %[sxw], 32\n\t"
      "s_nop 0\n\t"
      "buffer_load_dwordx4 %[xv], %[srdx], 0 offen offset:2048 lds\n\t"
      "v_lshl_or_b32 v130, v151, 16, v150\n\t"
      "v_lshl_or_b32 v131, v153, 16, v152\n\t"
      "s_add_u32 m0, %[sxw], 48\n\t"
      "s_nop 0\n\t"
      "buffer_load_dwordx4 %[xv], %[srdx], 0 offen offset:3072 lds\n\t"
      "v_add_u32 %[xv], 0x4000, %[xv]\n\t"
      "v_lshl_or_b32 v132, v155, 16, v154\n\t"
      "v_lshl_or_b32 v133, v157, 16, v156\n\t"
      "s_add_u32 m0, %[sxw], 18816\n\t"
      "s_nop 0\n\t"
      "buffer_load_dwordx4 %[xv], %[srdx], 0 offen offset:0 lds\n\t"
      "v_lshl_or_b32 v134, v159, 16, v158\n\t"
      "v_lshl_or_b32 v135, v161, 16, v160\n\t"
      "s_add_u32 m0, %[sxw], 18832\n\t"
      "s_nop 0\n\t"
      "buffer_load_dwordx4 %[xv], %[srdx], 0 offen offset:1024 lds\n\t"
      "v_lshl_or_b32 v136, v163, 16, v162\n\t"
      "s_add_u32 m0, %[sxw], 18848\n\t"
      "s_nop 0\n\t"
      "buffer_load_dwordx4 %[xv], %[srdx], 0 offen offset:2048 lds\n\t"
      "s_add_u32 m0, %[sxw], 18864\n\t"
      "s_nop 0\n\t"
      "buffer_load_dwordx4 %[xv], %[srdx], 0 offen offset:3072 lds\n\t"
      "v_add_u32 %[xv], 0x4000, %[xv]\n\t"
      "buffer_load_dword v190, %[pfx], %[srdx], 0 offen\n\t"
      "v_add_u32 %[pfx], 0x8000, %[pfx]\n\t"
      "s_waitcnt vmcnt(9) lgkmcnt(0)\n\t"
      "s_barrier\n\t"
      "v_add_u32 v174, %[shw], %[l16]\n\t"
      "ds_read_b128 v[178:181], v174 offset:56448\n\t"
      "v_mov_b32 v176, 2\n\t"
      "ds_write_b32 %[rw], v176\n\t"
      "ds_read_u16 v146, %[tb] offset:576\n\t"
      "ds_read_u16 v147, %[tb] offset:608\n\t"
      "ds_read_u16 v148, %[tb] offset:640\n\t"
      "ds_read_u16 v149, %[tb] offset:672\n\t"
      "s_add_u32 m0, %[sww], 24576\n\t"
      "s_mov_b32 s88, 0\n\t"
      "buffer_load_dwordx4 %[w2v], %[srdw], s88 offen lds\n\t"
      "ds_read_u16 v150, %[tb] offset:704\n\t"
      "ds_read_u16 v151, %[tb] offset:736\n\t"
      "ds_read_u16 v152, %[tb] offset:768\n\t"
      "ds_read_u16 v153, %[tb] offset:800\n\t"
      "s_add_u32 m0, %[sww], 25600\n\t"
      "s_mov_b32 s88, 4096\n\t"
      "buffer_load_dwordx4 %[w2v], %[srdw], s88 offen lds\n\t"
      "ds_read_u16 v154, %[tb] offset:832\n\t"
      "ds_read_u16 v155, %[tb] offset:864\n\t"
      "ds_read_u16 v156, %[tb] offset:896\n\t"
      "ds_read_u16 v157, %[tb] offset:928\n\t"
      "s_add_u32 m0, %[sww], 32768\n\t"
      "s_mov_b32 s88, 32768\n\t"
      "buffer_load_dwordx4 %[w2v], %[srdw], s88 offen lds\n\t"
      "ds_read_u16 v158, %[tb] offset:960\n\t"
      "ds_read_u16 v159, %[tb] offset:992\n\t"
      "ds_read_u16 v160, %[tb] offset:1024\n\t"
      "ds_read_u16 v161, %[tb] offset:1056\n\t"
      "s_add_u32 m0, %[sww], 33792\n\t"
      "s_mov_b32 s88, 36864\n\t"
      "buffer_load_dwordx4 %[w2v], %[srdw], s88 offen lds\n\t"
      "ds_read_u16 v162, %[tb] offset:1088\n\t"
      "ds_read_u16 v163, %[tb] offset:1120\n\t"
      "s_add_u32 m0, %[sxw], 37632\n\t"
      "s_nop 0\n\t"
      "buffer_load_dwordx4 %[xv], %[srdx], 0 offen offset:0 lds\n\t"
      "s_add_u32 m0, %[sxw], 37648\n\t"
      "s_nop 0\n\t"
      "buffer_load_dwordx4 %[xv], %[srdx], 0 offen offset:1024 lds\n\t"
      "s_waitcnt lgkmcnt(0)\n\t"
      "v_lshl_or_b32 v137, v147, 16, v146\n\t"
      "v_lshl_or_b32 v138, v149, 16, v148\n\t"
      "v_lshl_or_b32 v139, v151, 16, v150\n\t"
      "s_add_u32 m0, %[sxw], 37664\n\t"
      "s_nop 0\n\t"
      "buffer_load_dwordx4 %[xv], %[srdx], 0 offen offset:2048 lds\n\t"
      "v_lshl_or_b32 v140, v153, 16, v152\n\t"
      "v_lshl_or_b32 v141, v155, 16, v154\n\t"
      "v_lshl_or_b32 v142, v157, 16, v156\n\t"
      "s_add_u32 m0, %[sxw], 37680\n\t"
      "s_nop 0\n\t"
      "buffer_load_dwordx4 %[xv], %[srdx], 0 offen offset:3072 lds\n\t"
      "v_add_u32 %[xv], 0x4000, %[xv]\n\t"
      "v_lshl_or_b32 v143, v159, 16, v158\n\t"
      "v_lshl_or_b32 v144, v161, 16, v160\n\t"
      "v_lshl_or_b32 v145, v163, 16, v162\n\t"
      "s_mov_b32 exec_hi, 0\n\t"
      "buffer_store_dwordx4 v[178:181], %[l16], %[srds], %[sho] offen sc1\n\t"
      "s_mov_b64 exec, -1\n\t"
      "buffer_load_dword v190, %[pfx], %[srdx], 0 offen\n\t"
      "v_add_u32 %[pfx], 0x8000, %[pfx]\n\t"
      "s_mov_b32 s88, 131072\n\t"
      "buffer_load_dword v190, %[pfw], %[srdw], s88 offen\n\t"
      "s_waitcnt vmcnt(7) lgkmcnt(0)\n\t"
      "s_barrier\n\t"
      "ds_read_b128 v[146:149], %[h0] offset:37632\n\t"
      "ds_read_b128 v[150:153], %[h1] offset:37632\n\t"
      "ds_read_b128 v[154:157], %[h0] offset:56448\n\t"
      "ds_read_b128 v[158:161], %[h1] offset:56448\n\t"
      "ds_read_b128 v[162:165], %[h2] offset:0\n\t"
      "s_add_u32 m0, %[sxw], 56448\n\t"
      "s_nop 0\n\t"
      "buffer_load_dwordx4 %[xv], %[srdx], 0 offen offset:0 lds\n\t"
      "s_add_u32 m0, %[sxw], 56464\n\t"
      "s_nop 0\n\t"
      "buffer_load_dwordx4 %[xv], %[srdx], 0 offen offset:1024 lds\n\t"
      "s_add_u32 m0, %[sxw], 56480\n\t"
      "s_nop 0\n\t"
      "buffer_load_dwordx4 %[xv], %[srdx], 0 offen offset:2048 lds\n\t"
      "s_add_u32 m0, %[sxw], 56496\n\t"
      "s_nop 0\n\t"
      "buffer_load_dwordx4 %[xv], %[srdx], 0 offen offset:3072 lds\n\t"
      "v_add_u32 %[xv], 0x4000, %[xv]\n\t"
      "s_add_u32 m0, %[sww], 0\n\t"
      "s_mov_b32 s88, 65536\n\t"
      "buffer_load_dwordx4 %[w2v], %[srdw], s88 offen lds\n\t"
      "s_add_u32 m0, %[sww], 1024\n\t"
      "s_mov_b32 s88, 69632\n\t"
      "buffer_load_dwordx4 %[w2v], %[srdw], s88 offen lds\n\t"
      "s_add_u32 m0, %[sww], 8192\n\t"
      "s_mov_b32 s88, 98304\n\t"
      "buffer_load_dwordx4 %[w2v], %[srdw], s88 offen lds\n\t"
      "s_waitcnt lgkmcnt(0)\n\t"
      "s_setprio 1\n\t"
      "v_mfma_f32_4x4x4_16b_bf16 v[166:169], v[128:129], v[146:147], 0\n\t"
      "v_mfma_f32_4x4x4_16b_bf16 v[170:173], v[128:129], v[148:149], 0\n\t"
      "ds_read_b64_tr_b16 v[0:1], %[w2tr] offset:24576\n\t"
      "v_mfma_f32_4x4x4_16b_bf16 v[166:169], v[130:131], v[148:149], v[166:169]\n\t"
      "v_mfma_f32_4x4x4_16b_bf16 v[170:173], v[130:131], v[150:151], v[170:173]\n\t"
      "ds_read_b64_tr_b16 v[2:3], %[w2tr] offset:28672\n\t"
      "v_mfma_f32_4x4x4_16b_bf16 v[166:169], v[132:133], v[150:151], v[166:169]\n\t"
      "v_mfma_f32_4x4x4_16b_bf16 v[170:173], v[132:133], v[152:153], v[170:173]\n\t"
      "ds_read_b64_tr_b16 v[4:5], %[w2tr] offset:24584\n\t"
      "v_mfma_f32_4x4x4_16b_bf16 v[166:169], v[134:135], v[152:153], v[166:169]\n\t"
      "v_mfma_f32_4x4x4_16b_bf16 v[170:173], v[134:135], v[154:155], v[170:173]\n\t"
      "ds_read_b64_tr_b16 v[6:7], %[w2tr] offset:28680\n\t"
      "v_mfma_f32_4x4x4_16b_bf16 v[166:169], v[136:137], v[154:155], v[166:169]\n\t"
      "v_mfma_f32_4x4x4_16b_bf16 v[170:173], v[136:137], v[156:157], v[170:173]\n\t"
      "ds_read_b64_tr_b16 v[8:9], %[w2tr] offset:32768\n\t"
      "v_mfma_f32_4x4x4_16b_bf16 v[166:169], v[138:139], v[156:157], v[166:169]\n\t"
      "v_mfma_f32_4x4x4_16b_bf16 v[170:173], v[138:139], v[158:159], v[170:173]\n\t"
      "ds_read_b64_tr_b16 v[10:11], %[w2tr] offset:36864\n\t"
      "v_mfma_f32_4x4x4_16b_bf16 v[166:169], v[140:141], v[158:159], v[166:169]\n\t"
      "v_mfma_f32_4x4x4_16b_bf16 v[170:173], v[140:141], v[160:161], v[170:173]\n\t"
      "ds_read_b64_tr_b16 v[12:13], %[w2tr] offset:32776\n\t"
      "v_mfma_f32_4x4x4_16b_bf16 v[166:169], v[142:143], v[160:161], v[166:169]\n\t"
      "v_mfma_f32_4x4x4_16b_bf16 v[170:173], v[142:143], v[162:163], v[170:173]\n\t"
      "ds_read_b64_tr_b16 v[14:15], %[w2tr] offset:36872\n\t"
      "v_mfma_f32_4x4x4_16b_bf16 v[166:169], v[144:145], v[162:163], v[166:169]\n\t"
      "v_mfma_f32_4x4x4_16b_bf16 v[170:173], v[144:145], v[164:165], v[170:173]\n\t"
      "s_nop 0\n\t"
      "s_setprio 0\n\t"
      "s_nop 2\n\t"
      "s_add_u32 m0, %[sww], 9216\n\t"
      "s_mov_b32 s88, 102400\n\t"
      "buffer_load_dwordx4 %[w2v], %[srdw], s88 offen lds\n\t"
      "v_exp_f32 v174, v166\n\t"
      "v_exp_f32 v175, v167\n\t"
      "s_add_u32 m0, %[sxw], 75264\n\t"
      "s_nop 0\n\t"
      "buffer_load_dwordx4 %[xv], %[srdx], 0 offen offset:0 lds\n\t"
      "v_exp_f32 v176, v168\n\t"
      "v_exp_f32 v177, v169\n\t"
      "s_add_u32 m0, %[sxw], 75280\n\t"
      "s_nop 0\n\t"
      "buffer_load_dwordx4 %[xv], %[srdx], 0 offen offset:1024 lds\n\t"
      "v_fma_f32 v174, v174, %[k], %[k]\n\t"
      "v_fma_f32 v175, v175, %[k], %[k]\n\t"
      "s_add_u32 m0, %[sxw], 75296\n\t"
      "s_nop 0\n\t"
      "buffer_load_dwordx4 %[xv], %[srdx], 0 offen offset:2048 lds\n\t"
      "v_fma_f32 v176, v176, %[k], %[k]\n\t"
      "v_fma_f32 v177, v177, %[k], %[k]\n\t"
      "s_add_u32 m0, %[sxw], 75312\n\t"
      "s_nop 0\n\t"
      "buffer_load_dwordx4 %[xv], %[srdx], 0 offen offset:3072 lds\n\t"
      "v_add_u32 %[xv], 0x4000, %[xv]\n\t"
      "v_rcp_f32 v174, v174\n\t"
      "v_rcp_f32 v175, v175\n\t"
      "s_waitcnt vmcnt(12)\n\t"
      "s_mov_b32 exec_hi, 0\n\t"
      "buffer_load_dword v190, %[l16], %[srds], %[sho] offen sc1\n\t"
      "s_mov_b64 exec, -1\n\t"
      "buffer_load_dword v190, %[pfx], %[srdx], 0 offen\n\t"
      "v_add_u32 %[pfx], 0x8000, %[pfx]\n\t"
      "s_mov_b32 s88, 196608\n\t"
      "buffer_load_dword v190, %[pfw], %[srdw], s88 offen\n\t"
      "v_rcp_f32 v176, v176\n\t"
      "v_rcp_f32 v177, v177\n\t"
      "v_mul_f32 v174, v166, v174\n\t"
      "v_mul_f32 v175, v167, v175\n\t"
      "v_mul_f32 v176, v168, v176\n\t"
      "v_mul_f32 v177, v169, v177\n\t"
      "v_cvt_pk_bf16_f32 v178, v174, v175\n\t"
      "v_cvt_pk_bf16_f32 v179, v176, v177\n\t"
      "v_exp_f32 v174, v170\n\t"
      "v_exp_f32 v175, v171\n\t"
      "v_exp_f32 v176, v172\n\t"
      "v_exp_f32 v177, v173\n\t"
      "v_fma_f32 v174, v174, %[k], %[k]\n\t"
      "v_fma_f32 v175, v175, %[k], %[k]\n\t"
      "v_fma_f32 v176, v176, %[k], %[k]\n\t"
      "v_fma_f32 v177, v177, %[k], %[k]\n\t"
      "v_rcp_f32 v174, v174\n\t"
      "v_rcp_f32 v175, v175\n\t"
      "v_rcp_f32 v176, v176\n\t"
      "v_rcp_f32 v177, v177\n\t"
      "v_mul_f32 v174, v170, v174\n\t"
      "v_mul_f32 v175, v171, v175\n\t"
      "v_mul_f32 v176, v172, v176\n\t"
      "v_mul_f32 v177, v173, v177\n\t"
      "v_cvt_pk_bf16_f32 v180, v174, v175\n\t"
      "v_cvt_pk_bf16_f32 v181, v176, v177\n\t"
      "s_waitcnt vmcnt(7) lgkmcnt(0)\n\t"
      "s_barrier\n\t"
      "ds_read_b128 v[146:149], %[h2] offset:0\n\t"
      "ds_read_b128 v[150:153], %[h3] offset:0\n\t"
      "ds_read_b128 v[154:157], %[h4] offset:0\n\t"
      "ds_read_b128 v[158:161], %[h5] offset:0\n\t"
      "ds_read_b128 v[162:165], %[h0] offset:0\n\t"
      "s_add_u32 m0, %[sxw], 94080\n\t"
      "s_nop 0\n\t"
      "buffer_load_dwordx4 %[xv], %[srdx], 0 offen offset:0 lds\n\t"
      "s_add_u32 m0, %[sxw], 94096\n\t"
      "s_nop 0\n\t"
      "buffer_load_dwordx4 %[xv], %[srdx], 0 offen offset:1024 lds\n\t"
      "s_add_u32 m0, %[sxw], 94112\n\t"
      "s_nop 0\n\t"
      "buffer_load_dwordx4 %[xv], %[srdx], 0 offen offset:2048 lds\n\t"
      "s_add_u32 m0, %[sxw], 94128\n\t"
      "s_nop 0\n\t"
      "buffer_load_dwordx4 %[xv], %[srdx], 0 offen offset:3072 lds\n\t"
      "v_add_u32 %[xv], 0x4000, %[xv]\n\t"
      "s_add_u32 m0, %[sww], 24576\n\t"
      "s_mov_b32 s88, 131072\n\t"
      "buffer_load_dwordx4 %[w2v], %[srdw], s88 offen lds\n\t"
      "s_add_u32 m0, %[sww], 25600\n\t"
      "s_mov_b32 s88, 135168\n\t"
      "buffer_load_dwordx4 %[w2v], %[srdw], s88 offen lds\n\t"
      "s_add_u32 m0, %[sww], 32768\n\t"
      "s_mov_b32 s88, 163840\n\t"
      "buffer_load_dwordx4 %[w2v], %[srdw], s88 offen lds\n\t"
      "s_waitcnt lgkmcnt(0)\n\t"
      "s_setprio 1\n\t"
      "v_mfma_f32_4x4x4_16b_bf16 v[166:169], v[128:129], v[146:147], 0\n\t"
      "v_mfma_f32_4x4x4_16b_bf16 v[170:173], v[128:129], v[148:149], 0\n\t"
      "ds_read_b64_tr_b16 v[16:17], %[w2tr] offset:0\n\t"
      "v_mfma_f32_4x4x4_16b_bf16 v[166:169], v[130:131], v[148:149], v[166:169]\n\t"
      "v_mfma_f32_4x4x4_16b_bf16 v[170:173], v[130:131], v[150:151], v[170:173]\n\t"
      "ds_read_b64_tr_b16 v[18:19], %[w2tr] offset:4096\n\t"
      "v_mfma_f32_4x4x4_16b_bf16 v[166:169], v[132:133], v[150:151], v[166:169]\n\t"
      "v_mfma_f32_4x4x4_16b_bf16 v[170:173], v[132:133], v[152:153], v[170:173]\n\t"
      "ds_read_b64_tr_b16 v[20:21], %[w2tr] offset:8\n\t"
      "v_mfma_f32_4x4x4_16b_bf16 v[166:169], v[134:135], v[152:153], v[166:169]\n\t"
      "v_mfma_f32_4x4x4_16b_bf16 v[170:173], v[134:135], v[154:155], v[170:173]\n\t"
      "ds_read_b64_tr_b16 v[22:23], %[w2tr] offset:4104\n\t"
      "v_mfma_f32_4x4x4_16b_bf16 v[166:169], v[136:137], v[154:155], v[166:169]\n\t"
      "v_mfma_f32_4x4x4_16b_bf16 v[170:173], v[136:137], v[156:157], v[170:173]\n\t"
      "ds_read_b64_tr_b16 v[24:25], %[w2tr] offset:8192\n\t"
      "v_mfma_f32_4x4x4_16b_bf16 v[166:169], v[138:139], v[156:157], v[166:169]\n\t"
      "v_mfma_f32_4x4x4_16b_bf16 v[170:173], v[138:139], v[158:159], v[170:173]\n\t"
      "ds_read_b64_tr_b16 v[26:27], %[w2tr] offset:12288\n\t"
      "v_mfma_f32_4x4x4_16b_bf16 v[166:169], v[140:141], v[158:159], v[166:169]\n\t"
      "v_mfma_f32_4x4x4_16b_bf16 v[170:173], v[140:141], v[160:161], v[170:173]\n\t"
      "ds_read_b64_tr_b16 v[28:29], %[w2tr] offset:8200\n\t"
      "v_mfma_f32_4x4x4_16b_bf16 v[166:169], v[142:143], v[160:161], v[166:169]\n\t"
      "v_mfma_f32_4x4x4_16b_bf16 v[170:173], v[142:143], v[162:163], v[170:173]\n\t"
      "ds_read_b64_tr_b16 v[30:31], %[w2tr] offset:12296\n\t"
      "v_mfma_f32_4x4x4_16b_bf16 v[166:169], v[144:145], v[162:163], v[166:169]\n\t"
      "v_mfma_f32_4x4x4_16b_bf16 v[170:173], v[144:145], v[164:165], v[170:173]\n\t"
      "s_nop 0\n\t"
      "s_setprio 0\n\t"
      "s_nop 2\n\t"
      "s_add_u32 m0, %[sww], 33792\n\t"
      "s_mov_b32 s88, 167936\n\t"
      "buffer_load_dwordx4 %[w2v], %[srdw], s88 offen lds\n\t"
      "v_exp_f32 v174, v166\n\t"
      "v_exp_f32 v175, v167\n\t"
      "s_add_u32 m0, %[sxw], 0\n\t"
      "s_nop 0\n\t"
      "buffer_load_dwordx4 %[xv], %[srdx], 0 offen offset:0 lds\n\t"
      "v_exp_f32 v176, v168\n\t"
      "v_exp_f32 v177, v169\n\t"
      "s_add_u32 m0, %[sxw], 16\n\t"
      "s_nop 0\n\t"
      "buffer_load_dwordx4 %[xv], %[srdx], 0 offen offset:1024 lds\n\t"
      "v_fma_f32 v174, v174, %[k], %[k]\n\t"
      "v_fma_f32 v175, v175, %[k], %[k]\n\t"
      "s_add_u32 m0, %[sxw], 32\n\t"
      "s_nop 0\n\t"
      "buffer_load_dwordx4 %[xv], %[srdx], 0 offen offset:2048 lds\n\t"
      "v_fma_f32 v176, v176, %[k], %[k]\n\t"
      "v_fma_f32 v177, v177, %[k], %[k]\n\t"
      "s_add_u32 m0, %[sxw], 48\n\t"
      "s_nop 0\n\t"
      "buffer_load_dwordx4 %[xv], %[srdx], 0 offen offset:3072 lds\n\t"
      "v_add_u32 %[xv], 0x4000, %[xv]\n\t"
      "v_rcp_f32 v174, v174\n\t"
      "v_rcp_f32 v175, v175\n\t"
      "s_add_u32 s88, %[sst], 0\n\t"
      "buffer_store_dwordx4 v[178:181], %[sv], %[srds], s88 offen sc1\n\t"
      "buffer_load_dword v190, %[pfx], %[srdx], 0 offen\n\t"
      "v_add_u32 %[pfx], 0x8000, %[pfx]\n\t"
      "s_mov_b32 s88, 262144\n\t"
      "buffer_load_dword v190, %[pfw], %[srdw], s88 offen\n\t"
      "v_rcp_f32 v176, v176\n\t"
      "v_rcp_f32 v177, v177\n\t"
      "v_mul_f32 v174, v166, v174\n\t"
      "v_mul_f32 v175, v167, v175\n\t"
      "v_mul_f32 v176, v168, v176\n\t"
      "v_mul_f32 v177, v169, v177\n\t"
      "v_cvt_pk_bf16_f32 v178, v174, v175\n\t"
      "v_cvt_pk_bf16_f32 v179, v176, v177\n\t"
      "v_exp_f32 v174, v170\n\t"
      "v_exp_f32 v175, v171\n\t"
      "v_exp_f32 v176, v172\n\t"
      "v_exp_f32 v177, v173\n\t"
      "v_fma_f32 v174, v174, %[k], %[k]\n\t"
      "v_fma_f32 v175, v175, %[k], %[k]\n\t"
      "v_fma_f32 v176, v176, %[k], %[k]\n\t"
      "v_fma_f32 v177, v177, %[k], %[k]\n\t"
      "v_rcp_f32 v174, v174\n\t"
      "v_rcp_f32 v175, v175\n\t"
      "v_rcp_f32 v176, v176\n\t"
      "v_rcp_f32 v177, v177\n\t"
      "v_mul_f32 v174, v170, v174\n\t"
      "v_mul_f32 v175, v171, v175\n\t"
      "v_mul_f32 v176, v172, v176\n\t"
      "v_mul_f32 v177, v173, v177\n\t"
      "v_cvt_pk_bf16_f32 v180, v174, v175\n\t"
      "v_cvt_pk_bf16_f32 v181, v176, v177\n\t"
      "s_waitcnt vmcnt(7) lgkmcnt(0)\n\t"
      "s_barrier\n\t"
      "ds_read_b128 v[146:149], %[h0] offset:0\n\t"
      "ds_read_b128 v[150:153], %[h1] offset:0\n\t"
      "ds_read_b128 v[154:157], %[h0] offset:18816\n\t"
      "ds_read_b128 v[158:161], %[h1] offset:18816\n\t"
      "ds_read_b128 v[162:165], %[h0] offset:37632\n\t"
      "s_add_u32 m0, %[sxw], 18816\n\t"
      "s_nop 0\n\t"
      "buffer_load_dwordx4 %[xv], %[srdx], 0 offen offset:0 lds\n\t"
      "s_add_u32 m0, %[sxw], 18832\n\t"
      "s_nop 0\n\t"
      "buffer_load_dwordx4 %[xv], %[srdx], 0 offen offset:1024 lds\n\t"
      "s_add_u32 m0, %[sxw], 18848\n\t"
      "s_nop 0\n\t"
      "buffer_load_dwordx4 %[xv], %[srdx], 0 offen offset:2048 lds\n\t"
      "s_add_u32 m0, %[sxw], 18864\n\t"
      "s_nop 0\n\t"
      "buffer_load_dwordx4 %[xv], %[srdx], 0 offen offset:3072 lds\n\t"
      "v_add_u32 %[xv], 0x4000, %[xv]\n\t"
      "s_add_u32 m0, %[sww], 0\n\t"
      "s_mov_b32 s88, 196608\n\t"
      "buffer_load_dwordx4 %[w2v], %[srdw], s88 offen lds\n\t"
      "s_add_u32 m0, %[sww], 1024\n\t"
      "s_mov_b32 s88, 200704\n\t"
      "buffer_load_dwordx4 %[w2v], %[srdw], s88 offen lds\n\t"
      "s_add_u32 m0, %[sww], 8192\n\t"
      "s_mov_b32 s88, 229376\n\t"
      "buffer_load_dwordx4 %[w2v], %[srdw], s88 offen lds\n\t"
      "s_waitcnt lgkmcnt(0)\n\t"
      "s_setprio 1\n\t"
      "v_mfma_f32_4x4x4_16b_bf16 v[166:169], v[128:129], v[146:147], 0\n\t"
      "v_mfma_f32_4x4x4_16b_bf16 v[170:173], v[128:129], v[148:149], 0\n\t"
      "ds_read_b64_tr_b16 v[32:33], %[w2tr] offset:24576\n\t"
      "v_mfma_f32_4x4x4_16b_bf16 v[166:169], v[130:131], v[148:149], v[166:169]\n\t"
      "v_mfma_f32_4x4x4_16b_bf16 v[170:173], v[130:131], v[150:151], v[170:173]\n\t"
      "ds_read_b64_tr_b16 v[34:35], %[w2tr] offset:28672\n\t"
      "v_mfma_f32_4x4x4_16b_bf16 v[166:169], v[132:133], v[150:151], v[166:169]\n\t"
      "v_mfma_f32_4x4x4_16b_bf16 v[170:173], v[132:133], v[152:153], v[170:173]\n\t"
      "ds_read_b64_tr_b16 v[36:37], %[w2tr] offset:24584\n\t"
      "v_mfma_f32_4x4x4_16b_bf16 v[166:169], v[134:135], v[152:153], v[166:169]\n\t"
      "v_mfma_f32_4x4x4_16b_bf16 v[170:173], v[134:135], v[154:155], v[170:173]\n\t"
      "ds_read_b64_tr_b16 v[38:39], %[w2tr] offset:28680\n\t"
      "v_mfma_f32_4x4x4_16b_bf16 v[166:169], v[136:137], v[154:155], v[166:169]\n\t"
      "v_mfma_f32_4x4x4_16b_bf16 v[170:173], v[136:137], v[156:157], v[170:173]\n\t"
      "ds_read_b64_tr_b16 v[40:41], %[w2tr] offset:32768\n\t"
      "v_mfma_f32_4x4x4_16b_bf16 v[166:169], v[138:139], v[156:157], v[166:169]\n\t"
      "v_mfma_f32_4x4x4_16b_bf16 v[170:173], v[138:139], v[158:159], v[170:173]\n\t"
      "ds_read_b64_tr_b16 v[42:43], %[w2tr] offset:36864\n\t"
      "v_mfma_f32_4x4x4_16b_bf16 v[166:169], v[140:141], v[158:159], v[166:169]\n\t"
      "v_mfma_f32_4x4x4_16b_bf16 v[170:173], v[140:141], v[160:161], v[170:173]\n\t"
      "ds_read_b64_tr_b16 v[44:45], %[w2tr] offset:32776\n\t"
      "v_mfma_f32_4x4x4_16b_bf16 v[166:169], v[142:143], v[160:161], v[166:169]\n\t"
      "v_mfma_f32_4x4x4_16b_bf16 v[170:173], v[142:143], v[162:163], v[170:173]\n\t"
      "ds_read_b64_tr_b16 v[46:47], %[w2tr] offset:36872\n\t"
      "v_mfma_f32_4x4x4_16b_bf16 v[166:169], v[144:145], v[162:163], v[166:169]\n\t"
      "v_mfma_f32_4x4x4_16b_bf16 v[170:173], v[144:145], v[164:165], v[170:173]\n\t"
      "s_nop 0\n\t"
      "s_setprio 0\n\t"
      "s_nop 2\n\t"
      "s_add_u32 m0, %[sww], 9216\n\t"
      "s_mov_b32 s88, 233472\n\t"
      "buffer_load_dwordx4 %[w2v], %[srdw], s88 offen lds\n\t"
      "v_exp_f32 v174, v166\n\t"
      "v_exp_f32 v175, v167\n\t"
      "s_add_u32 m0, %[sxw], 37632\n\t"
      "s_nop 0\n\t"
      "buffer_load_dwordx4 %[xv], %[srdx], 0 offen offset:0 lds\n\t"
      "v_exp_f32 v176, v168\n\t"
      "v_exp_f32 v177, v169\n\t"
      "s_add_u32 m0, %[sxw], 37648\n\t"
      "s_nop 0\n\t"
      "buffer_load_dwordx4 %[xv], %[srdx], 0 offen offset:1024 lds\n\t"
      "v_fma_f32 v174, v174, %[k], %[k]\n\t"
      "v_fma_f32 v175, v175, %[k], %[k]\n\t"
      "s_add_u32 m0, %[sxw], 37664\n\t"
      "s_nop 0\n\t"
      "buffer_load_dwordx4 %[xv], %[srdx], 0 offen offset:2048 lds\n\t"
      "v_fma_f32 v176, v176, %[k], %[k]\n\t"
      "v_fma_f32 v177, v177, %[k], %[k]\n\t"
      "s_add_u32 m0, %[sxw], 37680\n\t"
      "s_nop 0\n\t"
      "buffer_load_dwordx4 %[xv], %[srdx], 0 offen offset:3072 lds\n\t"
      "v_add_u32 %[xv], 0x4000, %[xv]\n\t"
      "v_rcp_f32 v174, v174\n\t"
      "v_rcp_f32 v175, v175\n\t"
      "s_add_u32 s88, %[sst], 32768\n\t"
      "buffer_store_dwordx4 v[178:181], %[sv], %[srds], s88 offen sc1\n\t"
      "s_waitcnt vmcnt(13)\n\t"
      "s_add_u32 s88, %[sst], 0\n\t"
      "buffer_load_dword v190, %[sv], %[srds], s88 offen sc1\n\t"
      "s_mov_b32 s88, 327680\n\t"
      "buffer_load_dword v190, %[pfw], %[srdw], s88 offen\n\t"
      "s_mov_b32 s88, 425984\n\t"
      "buffer_load_dword v190, %[pfw], %[srdw], s88 offen\n\t"
      "v_rcp_f32 v176, v176\n\t"
      "v_rcp_f32 v177, v177\n\t"
      "v_mul_f32 v174, v166, v174\n\t"
      "v_mul_f32 v175, v167, v175\n\t"
      "v_mul_f32 v176, v168, v176\n\t"
      "v_mul_f32 v177, v169, v177\n\t"
      "v_cvt_pk_bf16_f32 v178, v174, v175\n\t"
      "v_cvt_pk_bf16_f32 v179, v176, v177\n\t"
      "v_exp_f32 v174, v170\n\t"
      "v_exp_f32 v175, v171\n\t"
      "v_exp_f32 v176, v172\n\t"
      "v_exp_f32 v177, v173\n\t"
      "v_fma_f32 v174, v174, %[k], %[k]\n\t"
      "v_fma_f32 v175, v175, %[k], %[k]\n\t"
      "v_fma_f32 v176, v176, %[k], %[k]\n\t"
      "v_fma_f32 v177, v177, %[k], %[k]\n\t"
      "v_rcp_f32 v174, v174\n\t"
      "v_rcp_f32 v175, v175\n\t"
      "v_rcp_f32 v176, v176\n\t"
      "v_rcp_f32 v177, v177\n\t"
      "v_mul_f32 v174, v170, v174\n\t"
      "v_mul_f32 v175, v171, v175\n\t"
      "v_mul_f32 v176, v172, v176\n\t"
      "v_mul_f32 v177, v173, v177\n\t"
      "v_cvt_pk_bf16_f32 v180, v174, v175\n\t"
      "v_cvt_pk_bf16_f32 v181, v176, v177\n\t"
      "s_waitcnt vmcnt(8) lgkmcnt(0)\n\t"
      "s_barrier\n\t"
      "ds_read_b128 v[146:149], %[h0] offset:37632\n\t"
      "ds_read_b128 v[150:153], %[h1] offset:37632\n\t"
      "ds_read_b128 v[154:157], %[h0] offset:56448\n\t"
      "ds_read_b128 v[158:161], %[h1] offset:56448\n\t"
      "ds_read_b128 v[162:165], %[h2] offset:0\n\t"
      "s_add_u32 m0, %[sxw], 56448\n\t"
      "s_nop 0\n\t"
      "buffer_load_dwordx4 %[xv], %[srdx], 0 offen offset:0 lds\n\t"
      "s_add_u32 m0, %[sxw], 56464\n\t"
      "s_nop 0\n\t"
      "buffer_load_dwordx4 %[xv], %[srdx], 0 offen offset:1024 lds\n\t"
      "s_add_u32 m0, %[sxw], 56480\n\t"
      "s_nop 0\n\t"
      "buffer_load_dwordx4 %[xv], %[srdx], 0 offen offset:2048 lds\n\t"
      "s_add_u32 m0, %[sxw], 56496\n\t"
      "s_nop 0\n\t"
      "buffer_load_dwordx4 %[xv], %[srdx], 0 offen offset:3072 lds\n\t"
      "v_add_u32 %[xv], 0x4000, %[xv]\n\t"
      "s_add_u32 m0, %[sww], 24576\n\t"
      "s_mov_b32 s88, 262144\n\t"
      "buffer_load_dwordx4 %[w2v], %[srdw], s88 offen lds\n\t"
      "s_add_u32 m0, %[sww], 25600\n\t"
      "s_mov_b32 s88, 266240\n\t"
      "buffer_load_dwordx4 %[w2v], %[srdw], s88 offen lds\n\t"
      "s_add_u32 m0, %[sww], 32768\n\t"
      "s_mov_b32 s88, 294912\n\t"
      "buffer_load_dwordx4 %[w2v], %[srdw], s88 offen lds\n\t"
      "s_waitcnt lgkmcnt(0)\n\t"
      "s_setprio 1\n\t"
      "v_mfma_f32_4x4x4_16b_bf16 v[166:169], v[128:129], v[146:147], 0\n\t"
      "v_mfma_f32_4x4x4_16b_bf16 v[170:173], v[128:129], v[148:149], 0\n\t"
      "ds_read_b64_tr_b16 v[48:49], %[w2tr] offset:0\n\t"
      "v_mfma_f32_4x4x4_16b_bf16 v[166:169], v[130:131], v[148:149], v[166:169]\n\t"
      "v_mfma_f32_4x4x4_16b_bf16 v[170:173], v[130:131], v[150:151], v[170:173]\n\t"
      "ds_read_b64_tr_b16 v[50:51], %[w2tr] offset:4096\n\t"
      "v_mfma_f32_4x4x4_16b_bf16 v[166:169], v[132:133], v[150:151], v[166:169]\n\t"
      "v_mfma_f32_4x4x4_16b_bf16 v[170:173], v[132:133], v[152:153], v[170:173]\n\t"
      "ds_read_b64_tr_b16 v[52:53], %[w2tr] offset:8\n\t"
      "v_mfma_f32_4x4x4_16b_bf16 v[166:169], v[134:135], v[152:153], v[166:169]\n\t"
      "v_mfma_f32_4x4x4_16b_bf16 v[170:173], v[134:135], v[154:155], v[170:173]\n\t"
      "ds_read_b64_tr_b16 v[54:55], %[w2tr] offset:4104\n\t"
      "v_mfma_f32_4x4x4_16b_bf16 v[166:169], v[136:137], v[154:155], v[166:169]\n\t"
      "v_mfma_f32_4x4x4_16b_bf16 v[170:173], v[136:137], v[156:157], v[170:173]\n\t"
      "ds_read_b64_tr_b16 v[56:57], %[w2tr] offset:8192\n\t"
      "v_mfma_f32_4x4x4_16b_bf16 v[166:169], v[138:139], v[156:157], v[166:169]\n\t"
      "v_mfma_f32_4x4x4_16b_bf16 v[170:173], v[138:139], v[158:159], v[170:173]\n\t"
      "ds_read_b64_tr_b16 v[58:59], %[w2tr] offset:12288\n\t"
      "v_mfma_f32_4x4x4_16b_bf16 v[166:169], v[140:141], v[158:159], v[166:169]\n\t"
      "v_mfma_f32_4x4x4_16b_bf16 v[170:173], v[140:141], v[160:161], v[170:173]\n\t"
      "ds_read_b64_tr_b16 v[60:61], %[w2tr] offset:8200\n\t"
      "v_mfma_f32_4x4x4_16b_bf16 v[166:169], v[142:143], v[160:161], v[166:169]\n\t"
      "v_mfma_f32_4x4x4_16b_bf16 v[170:173], v[142:143], v[162:163], v[170:173]\n\t"
      "ds_read_b64_tr_b16 v[62:63], %[w2tr] offset:12296\n\t"
      "v_mfma_f32_4x4x4_16b_bf16 v[166:169], v[144:145], v[162:163], v[166:169]\n\t"
      "v_mfma_f32_4x4x4_16b_bf16 v[170:173], v[144:145], v[164:165], v[170:173]\n\t"
      "s_nop 0\n\t"
      "s_setprio 0\n\t"
      "s_nop 2\n\t"
      "s_add_u32 m0, %[sww], 33792\n\t"
      "s_mov_b32 s88, 299008\n\t"
      "buffer_load_dwordx4 %[w2v], %[srdw], s88 offen lds\n\t"
      "v_exp_f32 v174, v166\n\t"
      "v_exp_f32 v175, v167\n\t"
      "s_add_u32 m0, %[sxw], 75264\n\t"
      "s_nop 0\n\t"
      "buffer_load_dwordx4 %[xv], %[srdx], 0 offen offset:0 lds\n\t"
      "v_exp_f32 v176, v168\n\t"
      "v_exp_f32 v177, v169\n\t"
      "s_add_u32 m0, %[sxw], 75280\n\t"
      "s_nop 0\n\t"
      "buffer_load_dwordx4 %[xv], %[srdx], 0 offen offset:1024 lds\n\t"
      "v_fma_f32 v174, v174, %[k], %[k]\n\t"
      "v_fma_f32 v175, v175, %[k], %[k]\n\t"
      "s_add_u32 m0, %[sxw], 75296\n\t"
      "s_nop 0\n\t"
      "buffer_load_dwordx4 %[xv], %[srdx], 0 offen offset:2048 lds\n\t"
      "v_fma_f32 v176, v176, %[k], %[k]\n\t"
      "v_fma_f32 v177, v177, %[k], %[k]\n\t"
      "s_add_u32 m0, %[sxw], 75312\n\t"
      "s_nop 0\n\t"
      "buffer_load_dwordx4 %[xv], %[srdx], 0 offen offset:3072 lds\n\t"
      "v_add_u32 %[xv], 0x4000, %[xv]\n\t"
      "v_rcp_f32 v174, v174\n\t"
      "v_rcp_f32 v175, v175\n\t"
      "s_add_u32 s88, %[sst], 65536\n\t"
      "buffer_store_dwordx4 v[178:181], %[sv], %[srds], s88 offen sc1\n\t"
      "s_waitcnt vmcnt(13)\n\t"
      "v_add_u32 v182, 2, %[tlo]\n\t"
      "v_mov_b32 v183, %[thi]\n\t"
      "s_mov_b64 exec, %[fm]\n\t"
      "global_atomic_swap_x2 %[fo], v[182:183], %[prog]\n\t"
      "s_mov_b64 exec, -1\n\t"
      "s_add_u32 s88, %[sst], 32768\n\t"
      "buffer_load_dword v190, %[sv], %[srds], s88 offen sc1\n\t"
      "v_rcp_f32 v176, v176\n\t"
      "v_rcp_f32 v177, v177\n\t"
      "v_mul_f32 v174, v166, v174\n\t"
      "v_mul_f32 v175, v167, v175\n\t"
      "v_mul_f32 v176, v168, v176\n\t"
      "v_mul_f32 v177, v169, v177\n\t"
      "v_cvt_pk_bf16_f32 v178, v174, v175\n\t"
      "v_cvt_pk_bf16_f32 v179, v176, v177\n\t"
      "v_exp_f32 v174, v170\n\t"
      "v_exp_f32 v175, v171\n\t"
      "v_exp_f32 v176, v172\n\t"
      "v_exp_f32 v177, v173\n\t"
      "v_fma_f32 v174, v174, %[k], %[k]\n\t"
      "v_fma_f32 v175, v175, %[k], %[k]\n\t"
      "v_fma_f32 v176, v176, %[k], %[k]\n\t"
      "v_fma_f32 v177, v177, %[k], %[k]\n\t"
      "v_rcp_f32 v174, v174\n\t"
      "v_rcp_f32 v175, v175\n\t"
      "v_rcp_f32 v176, v176\n\t"
      "v_rcp_f32 v177, v177\n\t"
      "v_mul_f32 v174, v170, v174\n\t"
      "v_mul_f32 v175, v171, v175\n\t"
      "v_mul_f32 v176, v172, v176\n\t"
      "v_mul_f32 v177, v173, v177\n\t"
      "v_cvt_pk_bf16_f32 v180, v174, v175\n\t"
      "v_cvt_pk_bf16_f32 v181, v176, v177\n\t"
      "s_waitcnt vmcnt(7) lgkmcnt(0)\n\t"
      "s_barrier\n\t"
      "ds_read_b128 v[146:149], %[h2] offset:0\n\t"
      "ds_read_b128 v[150:153], %[h3] offset:0\n\t"
      "ds_read_b128 v[154:157], %[h4] offset:0\n\t"
      "ds_read_b128 v[158:161], %[h5] offset:0\n\t"
      "ds_read_b128 v[162:165], %[h0] offset:0\n\t"
      "s_add_u32 m0, %[sww], 0\n\t"
      "s_mov_b32 s88, 327680\n\t"
      "buffer_load_dwordx4 %[w2v], %[srdw], s88 offen lds\n\t"
      "s_add_u32 m0, %[sww], 1024\n\t"
      "s_mov_b32 s88, 331776\n\t"
      "buffer_load_dwordx4 %[w2v], %[srdw], s88 offen lds\n\t"
      "s_add_u32 m0, %[sww], 8192\n\t"
      "s_mov_b32 s88, 360448\n\t"
      "buffer_load_dwordx4 %[w2v], %[srdw], s88 offen lds\n\t"
      "s_add_u32 m0, %[sww], 9216\n\t"
      "s_mov_b32 s88, 364544\n\t"
      "buffer_load_dwordx4 %[w2v], %[srdw], s88 offen lds\n\t"
      "s_add_u32 m0, %[sww], 16384\n\t"
      "s_mov_b32 s88, 393216\n\t"
      "buffer_load_dwordx4 %[w2v], %[srdw], s88 offen lds\n\t"
      "s_add_u32 m0, %[sww], 17408\n\t"
      "s_mov_b32 s88, 397312\n\t"
      "buffer_load_dwordx4 %[w2v], %[srdw], s88 offen lds\n\t"
      "s_add_u32 m0, %[sww], 40960\n\t"
      "s_mov_b32 s88, 425984\n\t"
      "buffer_load_dwordx4 %[w2v], %[srdw], s88 offen lds\n\t"
      "s_waitcnt lgkmcnt(0)\n\t"
      "s_setprio 1\n\t"
      "v_mfma_f32_4x4x4_16b_bf16 v[166:169], v[128:129], v[146:147], 0\n\t"
      "v_mfma_f32_4x4x4_16b_bf16 v[170:173], v[128:129], v[148:149], 0\n\t"
      "ds_read_b64_tr_b16 v[64:65], %[w2tr] offset:24576\n\t"
      "v_mfma_f32_4x4x4_16b_bf16 v[166:169], v[130:131], v[148:149], v[166:169]\n\t"
      "v_mfma_f32_4x4x4_16b_bf16 v[170:173], v[130:131], v[150:151], v[170:173]\n\t"
      "ds_read_b64_tr_b16 v[66:67], %[w2tr] offset:28672\n\t"
      "v_mfma_f32_4x4x4_16b_bf16 v[166:169], v[132:133], v[150:151], v[166:169]\n\t"
      "v_mfma_f32_4x4x4_16b_bf16 v[170:173], v[132:133], v[152:153], v[170:173]\n\t"
      "ds_read_b64_tr_b16 v[68:69], %[w2tr] offset:24584\n\t"
      "v_mfma_f32_4x4x4_16b_bf16 v[166:169], v[134:135], v[152:153], v[166:169]\n\t"
      "v_mfma_f32_4x4x4_16b_bf16 v[170:173], v[134:135], v[154:155], v[170:173]\n\t"
      "ds_read_b64_tr_b16 v[70:71], %[w2tr] offset:28680\n\t"
      "v_mfma_f32_4x4x4_16b_bf16 v[166:169], v[136:137], v[154:155], v[166:169]\n\t"
      "v_mfma_f32_4x4x4_16b_bf16 v[170:173], v[136:137], v[156:157], v[170:173]\n\t"
      "ds_read_b64_tr_b16 v[72:73], %[w2tr] offset:32768\n\t"
      "v_mfma_f32_4x4x4_16b_bf16 v[166:169], v[138:139], v[156:157], v[166:169]\n\t"
      "v_mfma_f32_4x4x4_16b_bf16 v[170:173], v[138:139], v[158:159], v[170:173]\n\t"
      "ds_read_b64_tr_b16 v[74:75], %[w2tr] offset:36864\n\t"
      "v_mfma_f32_4x4x4_16b_bf16 v[166:169], v[140:141], v[158:159], v[166:169]\n\t"
      "v_mfma_f32_4x4x4_16b_bf16 v[170:173], v[140:141], v[160:161], v[170:173]\n\t"
      "ds_read_b64_tr_b16 v[76:77], %[w2tr] offset:32776\n\t"
      "v_mfma_f32_4x4x4_16b_bf16 v[166:169], v[142:143], v[160:161], v[166:169]\n\t"
      "v_mfma_f32_4x4x4_16b_bf16 v[170:173], v[142:143], v[162:163], v[170:173]\n\t"
      "ds_read_b64_tr_b16 v[78:79], %[w2tr] offset:36872\n\t"
      "v_mfma_f32_4x4x4_16b_bf16 v[166:169], v[144:145], v[162:163], v[166:169]\n\t"
      "v_mfma_f32_4x4x4_16b_bf16 v[170:173], v[144:145], v[164:165], v[170:173]\n\t"
      "s_nop 0\n\t"
      "s_setprio 0\n\t"
      "s_nop 2\n\t"
      "s_add_u32 m0, %[sww], 41984\n\t"
      "s_mov_b32 s88, 430080\n\t"
      "buffer_load_dwordx4 %[w2v], %[srdw], s88 offen lds\n\t"
      "v_exp_f32 v174, v166\n\t"
      "v_exp_f32 v175, v167\n\t"
      "s_add_u32 s88, %[sst], 98304\n\t"
      "buffer_store_dwordx4 v[178:181], %[sv], %[srds], s88 offen sc1\n\t"
      "s_waitcnt vmcnt(9)\n\t"
      "v_add_u32 v182, 3, %[tlo]\n\t"
      "v_mov_b32 v183, %[thi]\n\t"
      "s_mov_b64 exec, 1\n\t"
      "global_atomic_umax_x2 %[fo], v[182:183], %[prog]\n\t"
      "s_mov_b64 exec, -1\n\t"
      "s_add_u32 s88, %[sst], 65536\n\t"
      "buffer_load_dword v190, %[sv], %[srds], s88 offen sc1\n\t"
      "v_exp_f32 v176, v168\n\t"
      "v_exp_f32 v177, v169\n\t"
      "v_fma_f32 v174, v174, %[k], %[k]\n\t"
      "v_fma_f32 v175, v175, %[k], %[k]\n\t"
      "v_fma_f32 v176, v176, %[k], %[k]\n\t"
      "v_fma_f32 v177, v177, %[k], %[k]\n\t"
      "v_rcp_f32 v174, v174\n\t"
      "v_rcp_f32 v175, v175\n\t"
      "v_rcp_f32 v176, v176\n\t"
      "v_rcp_f32 v177, v177\n\t"
      "v_mul_f32 v174, v166, v174\n\t"
      "v_mul_f32 v175, v167, v175\n\t"
      "v_mul_f32 v176, v168, v176\n\t"
      "v_mul_f32 v177, v169, v177\n\t"
      "v_cvt_pk_bf16_f32 v178, v174, v175\n\t"
      "v_cvt_pk_bf16_f32 v179, v176, v177\n\t"
      "v_exp_f32 v174, v170\n\t"
      "v_exp_f32 v175, v171\n\t"
      "v_exp_f32 v176, v172\n\t"
      "v_exp_f32 v177, v173\n\t"
      "v_fma_f32 v174, v174, %[k], %[k]\n\t"
      "v_fma_f32 v175, v175, %[k], %[k]\n\t"
      "v_fma_f32 v176, v176, %[k], %[k]\n\t"
      "v_fma_f32 v177, v177, %[k], %[k]\n\t"
      "v_rcp_f32 v174, v174\n\t"
      "v_rcp_f32 v175, v175\n\t"
      "v_rcp_f32 v176, v176\n\t"
      "v_rcp_f32 v177, v177\n\t"
      "v_mul_f32 v174, v170, v174\n\t"
      "v_mul_f32 v175, v171, v175\n\t"
      "v_mul_f32 v176, v172, v176\n\t"
      "v_mul_f32 v177, v173, v177\n\t"
      "v_cvt_pk_bf16_f32 v180, v174, v175\n\t"
      "v_cvt_pk_bf16_f32 v181, v176, v177\n\t"
      "s_add_u32 s88, %[sst], 131072\n\t"
      "buffer_store_dwordx4 v[178:181], %[sv], %[srds], s88 offen sc1\n\t"
      "s_waitcnt vmcnt(4) lgkmcnt(0)\n\t"
      "s_barrier\n\t"
      "ds_read_b128 v[146:149], %[h0] offset:0\n\t"
      "ds_read_b128 v[150:153], %[h1] offset:0\n\t"
      "ds_read_b128 v[154:157], %[h0] offset:18816\n\t"
      "ds_read_b128 v[158:161], %[h1] offset:18816\n\t"
      "ds_read_b128 v[162:165], %[h0] offset:37632\n\t"
      "s_add_u32 m0, %[sww], 24576\n\t"
      "s_mov_b32 s88, 458752\n\t"
      "buffer_load_dwordx4 %[w2v], %[srdw], s88 offen lds\n\t"
      "ds_read_b64_tr_b16 v[98:99], %[w2tr] offset:20480\n\t"
      "ds_read_b64_tr_b16 v[100:101], %[w2tr] offset:16392\n\t"
      "s_add_u32 m0, %[sww], 25600\n\t"
      "s_mov_b32 s88, 462848\n\t"
      "buffer_load_dwordx4 %[w2v], %[srdw], s88 offen lds\n\t"
      "ds_read_b64_tr_b16 v[102:103], %[w2tr] offset:20488\n\t"
      "ds_read_b64_tr_b16 v[104:105], %[w2tr] offset:40960\n\t"
      "s_add_u32 m0, %[sww], 32768\n\t"
      "s_mov_b32 s88, 491520\n\t"
      "buffer_load_dwordx4 %[w2v], %[srdw], s88 offen lds\n\t"
      "ds_read_b64_tr_b16 v[106:107], %[w2tr] offset:45056\n\t"
      "ds_read_b64_tr_b16 v[108:109], %[w2tr] offset:40968\n\t"
      "s_add_u32 m0, %[sww], 33792\n\t"
      "s_mov_b32 s88, 495616\n\t"
      "buffer_load_dwordx4 %[w2v], %[srdw], s88 offen lds\n\t"
      "ds_read_b64_tr_b16 v[110:111], %[w2tr] offset:45064\n\t"
      "s_waitcnt lgkmcnt(7)\n\t"
      "s_setprio 1\n\t"
      "v_mfma_f32_4x4x4_16b_bf16 v[166:169], v[128:129], v[146:147], 0\n\t"
      "v_mfma_f32_4x4x4_16b_bf16 v[170:173], v[128:129], v[148:149], 0\n\t"
      "ds_read_b64_tr_b16 v[80:81], %[w2tr] offset:0\n\t"
      "v_mfma_f32_4x4x4_16b_bf16 v[166:169], v[130:131], v[148:149], v[166:169]\n\t"
      "v_mfma_f32_4x4x4_16b_bf16 v[170:173], v[130:131], v[150:151], v[170:173]\n\t"
      "ds_read_b64_tr_b16 v[82:83], %[w2tr] offset:4096\n\t"
      "v_mfma_f32_4x4x4_16b_bf16 v[166:169], v[132:133], v[150:151], v[166:169]\n\t"
      "v_mfma_f32_4x4x4_16b_bf16 v[170:173], v[132:133], v[152:153], v[170:173]\n\t"
      "ds_read_b64_tr_b16 v[84:85], %[w2tr] offset:8\n\t"
      "v_mfma_f32_4x4x4_16b_bf16 v[166:169], v[134:135], v[152:153], v[166:169]\n\t"
      "v_mfma_f32_4x4x4_16b_bf16 v[170:173], v[134:135], v[154:155], v[170:173]\n\t"
      "ds_read_b64_tr_b16 v[86:87], %[w2tr] offset:4104\n\t"
      "v_mfma_f32_4x4x4_16b_bf16 v[166:169], v[136:137], v[154:155], v[166:169]\n\t"
      "v_mfma_f32_4x4x4_16b_bf16 v[170:173], v[136:137], v[156:157], v[170:173]\n\t"
      "ds_read_b64_tr_b16 v[88:89], %[w2tr] offset:8192\n\t"
      "v_mfma_f32_4x4x4_16b_bf16 v[166:169], v[138:139], v[156:157], v[166:169]\n\t"
      "v_mfma_f32_4x4x4_16b_bf16 v[170:173], v[138:139], v[158:159], v[170:173]\n\t"
      "ds_read_b64_tr_b16 v[90:91], %[w2tr] offset:12288\n\t"
      "v_mfma_f32_4x4x4_16b_bf16 v[166:169], v[140:141], v[158:159], v[166:169]\n\t"
      "v_mfma_f32_4x4x4_16b_bf16 v[170:173], v[140:141], v[160:161], v[170:173]\n\t"
      "ds_read_b64_tr_b16 v[92:93], %[w2tr] offset:8200\n\t"
      "v_mfma_f32_4x4x4_16b_bf16 v[166:169], v[142:143], v[160:161], v[166:169]\n\t"
      "v_mfma_f32_4x4x4_16b_bf16 v[170:173], v[142:143], v[162:163], v[170:173]\n\t"
      "ds_read_b64_tr_b16 v[94:95], %[w2tr] offset:12296\n\t"
      "v_mfma_f32_4x4x4_16b_bf16 v[166:169], v[144:145], v[162:163], v[166:169]\n\t"
      "v_mfma_f32_4x4x4_16b_bf16 v[170:173], v[144:145], v[164:165], v[170:173]\n\t"
      "ds_read_b64_tr_b16 v[96:97], %[w2tr] offset:16384\n\t"
      "s_setprio 0\n\t"
      "s_nop 2\n\t"
      "s_waitcnt vmcnt(4)\n\t"
      "v_add_u32 v182, 5, %[tlo]\n\t"
      "v_mov_b32 v183, %[thi]\n\t"
      "s_mov_b64 exec, 1\n\t"
      "global_atomic_umax_x2 %[fo], v[182:183], %[prog]\n\t"
      "s_mov_b64 exec, -1\n\t"
      "s_add_u32 s88, %[sst], 98304\n\t"
      "buffer_load_dword v190, %[sv], %[srds], s88 offen sc1\n\t"
      "s_add_u32 s88, %[sst], 131072\n\t"
      "buffer_load_dword v190, %[sv], %[srds], s88 offen sc1\n\t"
      "v_exp_f32 v174, v166\n\t"
      "v_exp_f32 v175, v167\n\t"
      "v_exp_f32 v176, v168\n\t"
      "v_exp_f32 v177, v169\n\t"
      "v_fma_f32 v174, v174, %[k], %[k]\n\t"
      "v_fma_f32 v175, v175, %[k], %[k]\n\t"
      "v_fma_f32 v176, v176, %[k], %[k]\n\t"
      "v_fma_f32 v177, v177, %[k], %[k]\n\t"
      "v_rcp_f32 v174, v174\n\t"
      "v_rcp_f32 v175, v175\n\t"
      "v_rcp_f32 v176, v176\n\t"
      "v_rcp_f32 v177, v177\n\t"
      "v_mul_f32 v174, v166, v174\n\t"
      "v_mul_f32 v175, v167, v175\n\t"
      "v_mul_f32 v176, v168, v176\n\t"
      "v_mul_f32 v177, v169, v177\n\t"
      "v_cvt_pk_bf16_f32 v178, v174, v175\n\t"
      "v_cvt_pk_bf16_f32 v179, v176, v177\n\t"
      "v_exp_f32 v174, v170\n\t"
      "v_exp_f32 v175, v171\n\t"
      "v_exp_f32 v176, v172\n\t"
      "v_exp_f32 v177, v173\n\t"
      "v_fma_f32 v174, v174, %[k], %[k]\n\t"
      "v_fma_f32 v175, v175, %[k], %[k]\n\t"
      "v_fma_f32 v176, v176, %[k], %[k]\n\t"
      "v_fma_f32 v177, v177, %[k], %[k]\n\t"
      "v_rcp_f32 v174, v174\n\t"
      "v_rcp_f32 v175, v175\n\t"
      "v_rcp_f32 v176, v176\n\t"
      "v_rcp_f32 v177, v177\n\t"
      "v_mul_f32 v174, v170, v174\n\t"
      "v_mul_f32 v175, v171, v175\n\t"
      "v_mul_f32 v176, v172, v176\n\t"
      "v_mul_f32 v177, v173, v177\n\t"
      "v_cvt_pk_bf16_f32 v180, v174, v175\n\t"
      "v_cvt_pk_bf16_f32 v181, v176, v177\n\t"
      "s_add_u32 s88, %[sst], 163840\n\t"
      "buffer_store_dwordx4 v[178:181], %[sv], %[srds], s88 offen sc1\n\t"
      "s_waitcnt vmcnt(4) lgkmcnt(0)\n\t"
      "s_barrier\n\t"
      "s_setprio 1\n\t"
      "ds_read_b128 v[146:149], %[h0] offset:37632\n\t"
      "ds_read_b128 v[150:153], %[h1] offset:37632\n\t"
      "ds_read_b128 v[154:157], %[h0] offset:56448\n\t"
      "ds_read_b128 v[158:161], %[h1] offset:56448\n\t"
      "ds_read_b128 v[162:165], %[h2] offset:0\n\t"
      "s_waitcnt lgkmcnt(0)\n\t"
      "v_mfma_f32_4x4x4_16b_bf16 v[166:169], v[128:129], v[146:147], 0\n\t"
      "v_mfma_f32_4x4x4_16b_bf16 v[170:173], v[128:129], v[148:149], 0\n\t"
      "s_nop 0\n\t"
      "v_mfma_f32_4x4x4_16b_bf16 v[166:169], v[130:131], v[148:149], v[166:169]\n\t"
      "v_mfma_f32_4x4x4_16b_bf16 v[170:173], v[130:131], v[150:151], v[170:173]\n\t"
      "s_nop 0\n\t"
      "v_mfma_f32_4x4x4_16b_bf16 v[166:169], v[132:133], v[150:151], v[166:169]\n\t"
      "v_mfma_f32_4x4x4_16b_bf16 v[170:173], v[132:133], v[152:153], v[170:173]\n\t"
      "s_nop 0\n\t"
      "v_mfma_f32_4x4x4_16b_bf16 v[166:169], v[134:135], v[152:153], v[166:169]\n\t"
      "v_mfma_f32_4x4x4_16b_bf16 v[170:173], v[134:135], v[154:155], v[170:173]\n\t"
      "s_nop 0\n\t"
      "v_mfma_f32_4x4x4_16b_bf16 v[166:169], v[136:137], v[154:155], v[166:169]\n\t"
      "v_mfma_f32_4x4x4_16b_bf16 v[170:173], v[136:137], v[156:157], v[170:173]\n\t"
      "s_nop 0\n\t"
      "v_mfma_f32_4x4x4_16b_bf16 v[166:169], v[138:139], v[156:157], v[166:169]\n\t"
      "v_mfma_f32_4x4x4_16b_bf16 v[170:173], v[138:139], v[158:159], v[170:173]\n\t"
      "s_nop 0\n\t"
      "v_mfma_f32_4x4x4_16b_bf16 v[166:169], v[140:141], v[158:159], v[166:169]\n\t"
      "v_mfma_f32_4x4x4_16b_bf16 v[170:173], v[140:141], v[160:161], v[170:173]\n\t"
      "s_nop 0\n\t"
      "v_mfma_f32_4x4x4_16b_bf16 v[166:169], v[142:143], v[160:161], v[166:169]\n\t"
      "v_mfma_f32_4x4x4_16b_bf16 v[170:173], v[142:143], v[162:163], v[170:173]\n\t"
      "s_nop 0\n\t"
      "v_mfma_f32_4x4x4_16b_bf16 v[166:169], v[144:145], v[162:163], v[166:169]\n\t"
      "v_mfma_f32_4x4x4_16b_bf16 v[170:173], v[144:145], v[164:165], v[170:173]\n\t"
      "s_nop 0\n\t"
      "s_setprio 0\n\t"
      "ds_read_b64_tr_b16 v[112:113], %[w2tr] offset:24576\n\t"
      "ds_read_b64_tr_b16 v[114:115], %[w2tr] offset:28672\n\t"
      "ds_read_b64_tr_b16 v[116:117], %[w2tr] offset:24584\n\t"
      "ds_read_b64_tr_b16 v[118:119], %[w2tr] offset:28680\n\t"
      "ds_read_b64_tr_b16 v[120:121], %[w2tr] offset:32768\n\t"
      "ds_read_b64_tr_b16 v[122:123], %[w2tr] offset:36864\n\t"
      "ds_read_b64_tr_b16 v[124:125], %[w2tr] offset:32776\n\t"
      "ds_read_b64_tr_b16 v[126:127], %[w2tr] offset:36872\n\t"
      "v_exp_f32 v174, v166\n\t"
      "v_exp_f32 v175, v167\n\t"
      "v_exp_f32 v176, v168\n\t"
      "v_exp_f32 v177, v169\n\t"
      "v_exp_f32 v182, v170\n\t"
      "v_exp_f32 v183, v171\n\t"
      "v_exp_f32 v188, v172\n\t"
      "v_exp_f32 v189, v173\n\t"
      "v_fma_f32 v174, v174, %[k], %[k]\n\t"
      "v_fma_f32 v175, v175, %[k], %[k]\n\t"
      "v_fma_f32 v176, v176, %[k], %[k]\n\t"
      "v_fma_f32 v177, v177, %[k], %[k]\n\t"
      "v_fma_f32 v182, v182, %[k], %[k]\n\t"
      "v_fma_f32 v183, v183, %[k], %[k]\n\t"
      "v_fma_f32 v188, v188, %[k], %[k]\n\t"
      "v_fma_f32 v189, v189, %[k], %[k]\n\t"
      "v_rcp_f32 v174, v174\n\t"
      "v_rcp_f32 v175, v175\n\t"
      "v_rcp_f32 v176, v176\n\t"
      "v_rcp_f32 v177, v177\n\t"
      "v_rcp_f32 v182, v182\n\t"
      "v_rcp_f32 v183, v183\n\t"
      "v_rcp_f32 v188, v188\n\t"
      "v_rcp_f32 v189, v189\n\t"
      "v_mul_f32 v174, v166, v174\n\t"
      "v_mul_f32 v175, v167, v175\n\t"
      "v_mul_f32 v176, v168, v176\n\t"
      "v_mul_f32 v177, v169, v177\n\t"
      "v_mul_f32 v182, v170, v182\n\t"
      "v_mul_f32 v183, v171, v183\n\t"
      "v_mul_f32 v188, v172, v188\n\t"
      "v_mul_f32 v189, v173, v189\n\t"
      "v_cvt_pk_bf16_f32 v184, v174, v175\n\t"
      "v_cvt_pk_bf16_f32 v185, v176, v177\n\t"
      "v_cvt_pk_bf16_f32 v186, v182, v183\n\t"
      "v_cvt_pk_bf16_f32 v187, v188, v189\n\t"
      "s_barrier\n\t"
      "ds_read_b128 v[146:149], %[h2] offset:0\n\t"
      "ds_read_b128 v[150:153], %[h3] offset:0\n\t"
      "ds_read_b128 v[154:157], %[h4] offset:0\n\t"
      "ds_read_b128 v[158:161], %[h5] offset:0\n\t"
      "ds_read_b128 v[162:165], %[h0] offset:0\n\t"
      "s_waitcnt vmcnt(0) lgkmcnt(0)\n\t"
      "v_mfma_f32_4x4x4_16b_bf16 v[166:169], v[128:129], v[146:147], 0\n\t"
      "v_mfma_f32_4x4x4_16b_bf16 v[170:173], v[128:129], v[148:149], 0\n\t"
      "s_nop 0\n\t"
      "v_mfma_f32_4x4x4_16b_bf16 v[166:169], v[130:131], v[148:149], v[166:169]\n\t"
      "v_mfma_f32_4x4x4_16b_bf16 v[170:173], v[130:131], v[150:151], v[170:173]\n\t"
      "s_nop 0\n\t"
      "v_mfma_f32_4x4x4_16b_bf16 v[166:169], v[132:133], v[150:151], v[166:169]\n\t"
      "v_mfma_f32_4x4x4_16b_bf16 v[170:173], v[132:133], v[152:153], v[170:173]\n\t"
      "s_nop 0\n\t"
      "v_mfma_f32_4x4x4_16b_bf16 v[166:169], v[134:135], v[152:153], v[166:169]\n\t"
      "v_mfma_f32_4x4x4_16b_bf16 v[170:173], v[134:135], v[154:155], v[170:173]\n\t"
      "s_nop 0\n\t"
      "v_mfma_f32_4x4x4_16b_bf16 v[166:169], v[136:137], v[154:155], v[166:169]\n\t"
      "v_mfma_f32_4x4x4_16b_bf16 v[170:173], v[136:137], v[156:157], v[170:173]\n\t"
      "s_nop 0\n\t"
      "v_mfma_f32_4x4x4_16b_bf16 v[166:169], v[138:139], v[156:157], v[166:169]\n\t"
      "v_mfma_f32_4x4x4_16b_bf16 v[170:173], v[138:139], v[158:159], v[170:173]\n\t"
      "s_nop 0\n\t"
      "v_mfma_f32_4x4x4_16b_bf16 v[166:169], v[140:141], v[158:159], v[166:169]\n\t"
      "v_mfma_f32_4x4x4_16b_bf16 v[170:173], v[140:141], v[160:161], v[170:173]\n\t"
      "s_nop 0\n\t"
      "v_mfma_f32_4x4x4_16b_bf16 v[166:169], v[142:143], v[160:161], v[166:169]\n\t"
      "v_mfma_f32_4x4x4_16b_bf16 v[170:173], v[142:143], v[162:163], v[170:173]\n\t"
      "s_nop 0\n\t"
      "v_mfma_f32_4x4x4_16b_bf16 v[166:169], v[144:145], v[162:163], v[166:169]\n\t"
      "v_mfma_f32_4x4x4_16b_bf16 v[170:173], v[144:145], v[164:165], v[170:173]\n\t"
      "s_nop 0\n\t"
      "s_add_u32 s88, %[sst], 196608\n\t"
      "buffer_store_dwordx4 v[184:187], %[sv], %[srds], s88 offen sc1\n\t"
      "v_add_u32 v182, 6, %[tlo]\n\t"
      "v_mov_b32 v183, %[thi]\n\t"
      "s_mov_b64 exec, 1\n\t"
      "global_atomic_umax_x2 %[fo], v[182:183], %[prog]\n\t"
      "s_mov_b64 exec, -1\n\t"
      "s_barrier\n\t"
      "ds_read_b64_tr_b16 v[128:129], %[st] offset:0\n\t"
      "ds_read_b64_tr_b16 v[130:131], %[st] offset:512\n\t"
      "ds_read_b64_tr_b16 v[132:133], %[st] offset:1024\n\t"
      "ds_read_b64_tr_b16 v[134:135], %[st] offset:1536\n\t"
      "ds_read_b64_tr_b16 v[136:137], %[st] offset:2048\n\t"
      "ds_read_b64_tr_b16 v[138:139], %[st] offset:2560\n\t"
      "ds_read_b64_tr_b16 v[140:141], %[st] offset:3072\n\t"
      "ds_read_b64_tr_b16 v[142:143], %[st] offset:3584\n\t"
      "ds_read_b64_tr_b16 v[144:145], %[st] offset:4096\n\t"
      "ds_read_b64_tr_b16 v[146:147], %[st] offset:4608\n\t"
      "ds_read_b64_tr_b16 v[148:149], %[st] offset:5120\n\t"
      "ds_read_b64_tr_b16 v[150:151], %[st] offset:5632\n\t"
      "v_exp_f32 v174, v166\n\t"
      "v_exp_f32 v175, v167\n\t"
      "v_exp_f32 v176, v168\n\t"
      "v_exp_f32 v177, v169\n\t"
      "v_exp_f32 v182, v170\n\t"
      "v_exp_f32 v183, v171\n\t"
      "v_exp_f32 v188, v172\n\t"
      "v_exp_f32 v189, v173\n\t"
      "v_fma_f32 v174, v174, %[k], %[k]\n\t"
      "v_fma_f32 v175, v175, %[k], %[k]\n\t"
      "v_fma_f32 v176, v176, %[k], %[k]\n\t"
      "v_fma_f32 v177, v177, %[k], %[k]\n\t"
      "v_fma_f32 v182, v182, %[k], %[k]\n\t"
      "v_fma_f32 v183, v183, %[k], %[k]\n\t"
      "v_fma_f32 v188, v188, %[k], %[k]\n\t"
      "v_fma_f32 v189, v189, %[k], %[k]\n\t"
      "v_rcp_f32 v174, v174\n\t"
      "v_rcp_f32 v175, v175\n\t"
      "v_rcp_f32 v176, v176\n\t"
      "v_rcp_f32 v177, v177\n\t"
      "v_rcp_f32 v182, v182\n\t"
      "v_rcp_f32 v183, v183\n\t"
      "v_rcp_f32 v188, v188\n\t"
      "v_rcp_f32 v189, v189\n\t"
      "v_mul_f32 v174, v166, v174\n\t"
      "v_mul_f32 v175, v167, v175\n\t"
      "v_mul_f32 v176, v168, v176\n\t"
      "v_mul_f32 v177, v169, v177\n\t"
      "v_mul_f32 v182, v170, v182\n\t"
      "v_mul_f32 v183, v171, v183\n\t"
      "v_mul_f32 v188, v172, v188\n\t"
      "v_mul_f32 v189, v173, v189\n\t"
      "v_cvt_pk_bf16_f32 v178, v174, v175\n\t"
      "v_cvt_pk_bf16_f32 v179, v176, v177\n\t"
      "v_cvt_pk_bf16_f32 v180, v182, v183\n\t"
      "v_cvt_pk_bf16_f32 v181, v188, v189\n\t"
      "s_add_u32 s88, %[sst], 229376\n\t"
      "buffer_store_dwordx4 v[178:181], %[sv], %[srds], s88 offen sc1\n\t"
      "s_mov_b32 s89, 8\n\t"
      "s_mov_b32 s88, 2\n\t"
      "s_waitcnt lgkmcnt(8)\n\t"
      ".p2align 5\n\t"
      "1:\n\t"
      "v_mfma_f32_16x16x32_bf16 v[160:163], v[0:3], v[128:131], 0\n\t"
      "ds_read_b64_tr_b16 v[152:153], %[st] offset:6144\n\t"
      "ds_read_b64_tr_b16 v[154:155], %[st] offset:6656\n\t"
      "v_mfma_f32_16x16x32_bf16 v[164:167], v[4:7], v[128:131], 0\n\t"
      "s_cmp_eq_u32 s89, 7\n\t"
      "s_cbranch_scc1 4f\n\t"
      "5:\n\t"
      "v_mfma_f32_16x16x32_bf16 v[160:163], v[8:11], v[132:135], v[160:163]\n\t"
      "ds_read_b64_tr_b16 v[156:157], %[st] offset:7168\n\t"
      "ds_read_b64_tr_b16 v[158:159], %[st] offset:7680\n\t"
      "v_mfma_f32_16x16x32_bf16 v[164:167], v[12:15], v[132:135], v[164:167]\n\t"
      "ds_read_b128 v[176:179], %[rp]\n\t"
      "s_waitcnt lgkmcnt(8)\n\t"
      "v_mfma_f32_16x16x32_bf16 v[160:163], v[16:19], v[136:139], v[160:163]\n\t"
      "ds_read_b64_tr_b16 v[128:129], %[st] offset:8192\n\t"
      "ds_read_b64_tr_b16 v[130:131], %[st] offset:8704\n\t"
      "v_mfma_f32_16x16x32_bf16 v[164:167], v[20:23], v[136:139], v[164:167]\n\t"
      "v_cvt_pk_bf16_f32 v184, v168, v169\n\t"
      "v_cvt_pk_bf16_f32 v185, v170, v171\n\t"
      "v_mfma_f32_16x16x32_bf16 v[160:163], v[24:27], v[140:143], v[160:163]\n\t"
      "ds_read_b64_tr_b16 v[132:133], %[st] offset:9216\n\t"
      "ds_read_b64_tr_b16 v[134:135], %[st] offset:9728\n\t"
      "v_mfma_f32_16x16x32_bf16 v[164:167], v[28:31], v[140:143], v[164:167]\n\t"
      "v_cvt_pk_bf16_f32 v186, v172, v173\n\t"
      "s_waitcnt lgkmcnt(8)\n\t"
      "v_mfma_f32_16x16x32_bf16 v[160:163], v[32:35], v[144:147], v[160:163]\n\t"
      "ds_read_b64_tr_b16 v[136:137], %[st] offset:10240\n\t"
      "ds_read_b64_tr_b16 v[138:139], %[st] offset:10752\n\t"
      "v_mfma_f32_16x16x32_bf16 v[164:167], v[36:39], v[144:147], v[164:167]\n\t"
      "v_cvt_pk_bf16_f32 v187, v174, v175\n\t"
      "s_sub_u32 s89, s89, 1\n\t"
      "v_mfma_f32_16x16x32_bf16 v[160:163], v[40:43], v[148:151], v[160:163]\n\t"
      "ds_read_b64_tr_b16 v[140:141], %[st] offset:11264\n\t"
      "ds_read_b64_tr_b16 v[142:143], %[st] offset:11776\n\t"
      "v_mfma_f32_16x16x32_bf16 v[164:167], v[44:47], v[148:151], v[164:167]\n\t"
      "buffer_store_dwordx4 v[184:187], %[ov], %[srdo], 0 offen sc1\n\t"
      "s_waitcnt lgkmcnt(8)\n\t"
      "v_mfma_f32_16x16x32_bf16 v[160:163], v[48:51], v[152:155], v[160:163]\n\t"
      "ds_read_b64_tr_b16 v[144:145], %[st] offset:12288\n\t"
      "ds_read_b64_tr_b16 v[146:147], %[st] offset:12800\n\t"
      "v_mfma_f32_16x16x32_bf16 v[164:167], v[52:55], v[152:155], v[164:167]\n\t"
      "v_min3_u32 v176, v176, v177, v178\n\t"
      "v_min_u32 v176, v176, v179\n\t"
      "v_mfma_f32_16x16x32_bf16 v[160:163], v[56:59], v[156:159], v[160:163]\n\t"
      "ds_read_b64_tr_b16 v[148:149], %[st] offset:13312\n\t"
      "ds_read_b64_tr_b16 v[150:151], %[st] offset:13824\n\t"
      "v_mfma_f32_16x16x32_bf16 v[164:167], v[60:63], v[156:159], v[164:167]\n\t"
      "s_waitcnt lgkmcnt(8)\n\t"
      "v_cmp_gt_u32 vcc, s88, v176\n\t"
      "v_mfma_f32_16x16x32_bf16 v[160:163], v[64:67], v[128:131], v[160:163]\n\t"
      "ds_read_b64_tr_b16 v[152:153], %[st] offset:14336\n\t"
      "ds_read_b64_tr_b16 v[154:155], %[st] offset:14848\n\t"
      "v_mfma_f32_16x16x32_bf16 v[164:167], v[68:71], v[128:131], v[164:167]\n\t"
      "s_waitcnt lgkmcnt(6)\n\t"
      "s_cmp_eq_u32 s89, 7\n\t"
      "v_mfma_f32_16x16x32_bf16 v[160:163], v[72:75], v[132:135], v[160:163]\n\t"
      "ds_read_b64_tr_b16 v[156:157], %[st] offset:15360\n\t"
      "ds_read_b64_tr_b16 v[158:159], %[st] offset:15872\n\t"
      "v_mfma_f32_16x16x32_bf16 v[164:167], v[76:79], v[132:135], v[164:167]\n\t"
      "v_add_u32 %[st], 18816, %[st]\n\t"
      "s_cbranch_vccnz 3f\n\t"
      "2:\n\t"
      "v_mfma_f32_16x16x32_bf16 v[160:163], v[80:83], v[136:139], v[160:163]\n\t"
      "ds_read_b64_tr_b16 v[128:129], %[st] offset:0\n\t"
      "ds_read_b64_tr_b16 v[130:131], %[st] offset:512\n\t"
      "v_mfma_f32_16x16x32_bf16 v[164:167], v[84:87], v[136:139], v[164:167]\n\t"
      "s_cbranch_scc1 50f\n\t"
      "51:\n\t"
      "v_mov_b32 %[ov], %[on]\n\t"
      "v_mfma_f32_16x16x32_bf16 v[160:163], v[88:91], v[140:143], v[160:163]\n\t"
      "ds_read_b64_tr_b16 v[132:133], %[st] offset:1024\n\t"
      "ds_read_b64_tr_b16 v[134:135], %[st] offset:1536\n\t"
      "v_mfma_f32_16x16x32_bf16 v[164:167], v[92:95], v[140:143], v[164:167]\n\t"
      "s_add_u32 s88, s88, 1\n\t"
      "s_waitcnt lgkmcnt(8)\n\t"
      "v_mfma_f32_16x16x32_bf16 v[160:163], v[96:99], v[144:147], v[160:163]\n\t"
      "ds_read_b64_tr_b16 v[136:137], %[st] offset:2048\n\t"
      "ds_read_b64_tr_b16 v[138:139], %[st] offset:2560\n\t"
      "v_mfma_f32_16x16x32_bf16 v[164:167], v[100:103], v[144:147], v[164:167]\n\t"
      "s_min_u32 s88, s88, 8\n\t"
      "v_add_u32 %[on], 0x8000, %[on]\n\t"
      "v_mfma_f32_16x16x32_bf16 v[160:163], v[104:107], v[148:151], v[160:163]\n\t"
      "ds_read_b64_tr_b16 v[140:141], %[st] offset:3072\n\t"
      "ds_read_b64_tr_b16 v[142:143], %[st] offset:3584\n\t"
      "v_mfma_f32_16x16x32_bf16 v[164:167], v[108:111], v[148:151], v[164:167]\n\t"
      "s_waitcnt lgkmcnt(8)\n\t"
      "s_cmp_eq_u32 s89, %[upd]\n\t"
      "v_mfma_f32_16x16x32_bf16 v[160:163], v[112:115], v[152:155], v[160:163]\n\t"
      "ds_read_b64_tr_b16 v[144:145], %[st] offset:4096\n\t"
      "ds_read_b64_tr_b16 v[146:147], %[st] offset:4608\n\t"
      "v_mfma_f32_16x16x32_bf16 v[164:167], v[116:119], v[152:155], v[164:167]\n\t"
      "s_cbranch_scc1 38f\n\t"
      "39:\n\t"
      "s_cmp_lg_u32 s89, 0\n\t"
      "v_mfma_f32_16x16x32_bf16 v[168:171], v[120:123], v[156:159], v[160:163]\n\t"
      "ds_read_b64_tr_b16 v[148:149], %[st] offset:5120\n\t"
      "ds_read_b64_tr_b16 v[150:151], %[st] offset:5632\n\t"
      "v_mfma_f32_16x16x32_bf16 v[172:175], v[124:127], v[156:159], v[164:167]\n\t"
      "s_waitcnt lgkmcnt(8)\n\t"
      "s_cbranch_scc1 1b\n\t"
      "s_nop 7\n\t"
      "v_cvt_pk_bf16_f32 v184, v168, v169\n\t"
      "v_cvt_pk_bf16_f32 v185, v170, v171\n\t"
      "v_cvt_pk_bf16_f32 v186, v172, v173\n\t"
      "v_cvt_pk_bf16_f32 v187, v174, v175\n\t"
      "buffer_store_dwordx4 v[184:187], %[ov], %[srdo], 0 offen sc1\n\t"
      "s_waitcnt lgkmcnt(0)\n\t"
      "s_branch 9f\n\t"
      "3:\n\t"
      "s_mov_b32 s86, 0x1000000\n\t"
      "6:\n\t"
      "ds_read_b128 v[176:179], %[rp]\n\t"
      "s_waitcnt lgkmcnt(0)\n\t"
      "v_min3_u32 v176, v176, v177, v178\n\t"
      "v_min_u32 v176, v176, v179\n\t"
      "v_cmp_gt_u32 vcc, s88, v176\n\t"
      "s_sub_u32 s86, s86, 1\n\t"
      "s_cbranch_scc1 61f\n\t"
      "s_cbranch_vccnz 6b\n\t"
      "61:\n\t"
      "s_cmp_eq_u32 s89, 7\n\t"
      "s_branch 2b\n\t"
      "4:\n\t"
      "v_add_u32 v180, 8, %[tlo]\n\t"
      "v_mov_b32 v181, %[thi]\n\t"
      "s_waitcnt vmcnt(2)\n\t"
      "s_mov_b64 exec, 1\n\t"
      "global_atomic_umax_x2 %[fo], v[180:181], %[prog]\n\t"
      "s_mov_b64 exec, -1\n\t"
      "s_branch 5b\n\t"
      "50:\n\t"
      "v_add_u32 v180, 7, %[tlo]\n\t"
      "v_mov_b32 v181, %[thi]\n\t"
      "s_waitcnt vmcnt(3)\n\t"
      "s_mov_b64 exec, 1\n\t"
      "global_atomic_umax_x2 %[fo], v[180:181], %[prog]\n\t"
      "s_mov_b64 exec, -1\n\t"
      "s_branch 51b\n\t"
      "38:\n\t"
      "v_mov_b32 v180, 1\n\t"
      "v_mov_b32 v181, 0\n\t"
      "s_mov_b64 exec, 1\n\t"
      "global_atomic_add_x2 v181, v[180:181], %[prog] offset:512\n\t"
      "s_mov_b64 exec, -1\n\t"
      "s_branch 39b\n\t"
      "9:\n\t"
      ".Lcm_end_b:\n\t"
      : [xv] "+v"(xv), [st] "+v"(st), [ov] "+v"(ov), [on] "+v"(ovn), [tlo] "=&v"(tlo), [thi] "=&v"(thi), [pfx] "+v"(pfx)
      : [h0] "v"(hA[0]), [h1] "v"(hA[1]), [h2] "v"(hA[2]), [h3] "v"(hA[3]), [h4] "v"(hA[4]), [h5] "v"(hA[5]), [w2v] "v"(w2v), [w2tr] "v"(w2tr), [sv] "v"(sv), [pfw] "v"(pfw), [rw] "v"(rw), [tb] "v"(tb), [fo] "v"(fo), [rp] "v"(rp), [l16] "v"(l16), [srdx] "s"(srd_xb), [srdw] "s"(srd_w2), [srds] "s"(srd_s), [srdo] "s"(srd_o), [prog] "s"(prog), [sxw] "s"(sxw), [sww] "s"(sww), [sst] "s"(sst), [k] "s"(nl2e), [upd] "s"(4 * j + w == 0 ? 1 : 99), [fm] "s"(fm), [shw] "s"(shw), [sho] "s"(sho)
      : "memory", "scc", "vcc", "s85", "s86", "s87", "s88", "s89", "s90", "s91", "v0", "v1", "v2", "v3", "v4", "v5", "v6", "v7", "v8", "v9", "v10", "v11", "v12", "v13", "v14", "v15", "v16", "v17", "v18", "v19", "v20", "v21", "v22", "v23", "v24", "v25", "v26", "v27", "v28", "v29", "v30", "v31", "v32", "v33", "v34", "v35", "v36", "v37", "v38", "v39", "v40", "v41", "v42", "v43", "v44", "v45", "v46", "v47", "v48", "v49", "v50", "v51", "v52", "v53", "v54", "v55", "v56", "v57", "v58", "v59", "v60", "v61", "v62", "v63", "v64", "v65", "v66", "v67", "v68", "v69", "v70", "v71", "v72", "v73", "v74", "v75", "v76", "v77", "v78", "v79", "v80", "v81", "v82", "v83", "v84", "v85", "v86", "v87", "v88", "v89", "v90", "v91", "v92", "v93", "v94", "v95", "v96", "v97", "v98", "v99", "v100", "v101", "v102", "v103", "v104", "v105", "v106", "v107", "v108", "v109", "v110", "v111", "v112", "v113", "v114", "v115", "v116", "v117", "v118", "v119", "v120", "v121", "v122", "v123", "v124", "v125", "v126", "v127", "v128", "v129", "v130", "v131", "v132", "v133", "v134", "v135", "v136", "v137", "v138", "v139", "v140", "v141", "v142", "v143", "v144", "v145", "v146", "v147", "v148", "v149", "v150", "v151", "v152", "v153", "v154", "v155", "v156", "v157", "v158", "v159", "v160", "v161", "v162", "v163", "v164", "v165", "v166", "v167", "v168", "v169", "v170", "v171", "v172", "v173", "v174", "v175", "v176", "v177", "v178", "v179", "v180", "v181", "v182", "v183", "v184", "v185", "v186", "v187", "v188", "v189", "v190");
    __builtin_amdgcn_endpgm();
  }
}

extern "C" void kernel_launch(void* const* d_in, const int* in_sizes, int n_in, void* d_out, int out_size,
                              void* d_ws, size_t ws_size, hipStream_t stream) {
  conv_module_fused<<<256, 512, 0, stream>>>((const unsigned short*)d_in[0], (const unsigned short*)d_in[1],
                                             (const unsigned short*)d_in[2], (const unsigned short*)d_in[3],
                                             (unsigned short*)d_out, (unsigned char*)d_ws);
}
```
